```python
import torch

def _softmax(x, axis=-1):
    return torch.softmax(x, dim=axis)

def _gelu(x):
    return torch.nn.functional.gelu(x, approximate='tanh')

def _silu(x):
    return torch.nn.functional.silu(x)

def _sigmoid(x):
    return torch.sigmoid(x)

def _layernorm(x, axis=-1, eps=1e-05):
    m = x.mean(dim=axis, keepdim=True)
    v = x.var(dim=axis, keepdim=True, unbiased=False)
    return (x - m) / torch.sqrt(v + eps)

def _scatter_add(tgt, idx, src, axis=0):
    return tgt.clone().index_add_(axis, idx, src)

def _segment_sum(src, seg, n):
    out = torch.zeros((n,) + src.shape[1:], dtype=src.dtype)
    return out.index_add_(0, seg, src)

def _pad(x, pw):
    flat = tuple((v for pair in reversed(pw) for v in pair))
    return torch.nn.functional.pad(x, flat)
kernel_name = 'adaLN_cond__memory_aug_recurrent_mem_tr__mixer_token_mlp__prompt'

def setup_inputs():

    _w = lambda a: (lambda t: t.to(torch.bfloat16) if t.is_floating_point() else t)(torch.as_tensor(a))

    def _A_setup():
        B, S, Dm = (1, 2048, 512)
        H, Dh = (32, 128)
        M, seg = (8, 32)
        seg = min(seg, S)
        S = S // seg * seg
        return {'x': _w((lambda *s: torch.randn(*s, dtype=torch.bfloat16) * 0.05)(B, S, Dm)), 'mem0': _w((lambda *s: torch.randn(*s, dtype=torch.bfloat16) * 0.4)(B, M, Dm)), 'Wq': _w((lambda *s: torch.randn(*s, dtype=torch.bfloat16) * 1.6)(Dm, H, Dh) * Dm ** (-0.5)), 'Wk': _w((lambda *s: torch.randn(*s, dtype=torch.bfloat16) * 1.6)(Dm, H, Dh) * Dm ** (-0.5)), 'Wv': _w((lambda *s: torch.randn(*s, dtype=torch.bfloat16) * 0.4)(Dm, H, Dh) * Dm ** (-0.5)), 'Wo': _w((lambda *s: torch.randn(*s, dtype=torch.bfloat16) * 0.4)(H, Dh, Dm) * (H * Dh) ** (-0.5)), 'seg_len': seg}
    _A_in = _A_setup()

    def _B_setup():
        B, S, D, F = (1, 2048, 512, 1792)
        return {'x': _w((lambda *s: torch.randn(*s, dtype=torch.bfloat16) * 0.05)(B, S, D)), 'w1': _w((lambda *s: torch.randn(*s, dtype=torch.bfloat16) * 0.8)(S, F) * S ** (-0.5)), 'b1': _w((lambda *s: torch.randn(*s, dtype=torch.bfloat16) * 12.8)(F) * 0.02), 'w2': _w((lambda *s: torch.randn(*s, dtype=torch.bfloat16) * 0.2)(F, S) * F ** (-0.5)), 'b2': _w((lambda *s: torch.randn(*s, dtype=torch.bfloat16) * 1.6)(S) * 0.02)}
    _B_in = _B_setup()
    return {'A_x': _A_in['x'], 'A_mem0': _A_in['mem0'], 'A_Wq': _A_in['Wq'], 'A_Wk': _A_in['Wk'], 'A_Wv': _A_in['Wv'], 'A_Wo': _A_in['Wo'], 'A_seg_len': _A_in['seg_len'], 'B_x': _B_in['x'], 'B_w1': _B_in['w1'], 'B_b1': _B_in['b1'], 'B_w2': _B_in['w2'], 'B_b2': _B_in['b2'], 'c': _w((lambda *s: torch.randn(*s, dtype=torch.bfloat16) * 0.1)(1, 512) * 8.0), 'w_mod': _w((lambda *s: torch.randn(*s, dtype=torch.bfloat16) * 0.1)(512, 6 * 512) * 512 ** (-0.5) * 8.0)}

def reference(A_x, A_mem0, A_Wq, A_Wk, A_Wv, A_Wo, A_seg_len, B_x, B_w1, B_b1, B_w2, B_b2, c, w_mod):

    def _A_ref(x, mem0, Wq, Wk, Wv, Wo, seg_len):
        B, S, Dm = x.shape
        H, Dh = (Wq.shape[1], Wq.shape[2])
        M = mem0.shape[1]
        scale = Dh ** (-0.5)
        n_seg = S // seg_len
        L = M + seg_len
        idx = torch.arange(L)
        mask = (idx[:, None] < M) | (idx[:, None] >= idx[None, :])
        mem = mem0
        outs = []
        for s in range(n_seg):
            xs = x[:, s * seg_len:(s + 1) * seg_len, :].clone()
            h = torch.concatenate([mem, xs], axis=1)
            q = torch.einsum('btd,dhe->bhte', h, Wq)
            k = torch.einsum('btd,dhe->bhte', h, Wk)
            v = torch.einsum('btd,dhe->bhte', h, Wv)
            scores = torch.einsum('bhqe,bhke->bhqk', q, k) * scale
            scores = torch.where(mask[None, None], scores, -1000000000.0)
            p = _softmax(scores, axis=-1)
            o = torch.einsum('bhqk,bhke->bhqe', p, v)
            o = torch.einsum('bhte,hed->btd', o, Wo)
            mem = o[:, :M, :].clone()
            outs.append(o[:, M:, :])
        return torch.concatenate(outs, axis=1)

    def _B_ref(x, w1, b1, w2, b2):
        B, S, D = x.shape
        h = _layernorm(x, axis=-1)
        u = torch.einsum('bsd,sf->bdf', h, w1) + b1
        u = _gelu(u)
        y = torch.einsum('bdf,fs->bds', u, w2) + b2
        return (lambda x, *d: x.permute(*d))(y, 0, 2, 1)
    x = A_x
    mod = (_silu(c) @ w_mod).reshape(c.shape[0], 6, 1, -1)
    sa, ca, ga, sb, cb, gb = (mod[:, 0].clone(), mod[:, 1].clone(), mod[:, 2].clone(), mod[:, 3].clone(), mod[:, 4].clone(), mod[:, 5].clone())
    h = _layernorm(x, axis=-1) * (1 + ca) + sa
    x = x + ga * _A_ref(h, A_mem0, A_Wq, A_Wk, A_Wv, A_Wo, A_seg_len)
    h = _layernorm(x, axis=-1) * (1 + cb) + sb
    x = x + gb * _B_ref(h, B_w1, B_b1, B_w2, B_b2)
    return x
```

```cpp
#include <hip/hip_runtime.h>
#include <hip/hip_bf16.h>
#include <stdint.h>

typedef unsigned short u16;
typedef __bf16 bf16x2 __attribute__((ext_vector_type(2)));
typedef __bf16 bf16x8 __attribute__((ext_vector_type(8)));
typedef __bf16 bf16x16 __attribute__((ext_vector_type(16)));
typedef short s16x4 __attribute__((ext_vector_type(4)));
typedef float f32x2 __attribute__((ext_vector_type(2)));
typedef float f32x4 __attribute__((ext_vector_type(4)));
typedef unsigned u32x2 __attribute__((ext_vector_type(2)));
typedef unsigned u32x4 __attribute__((ext_vector_type(4)));
typedef unsigned u32x8 __attribute__((ext_vector_type(8)));
#define LAS __attribute__((address_space(3)))
#define GAS __attribute__((address_space(1)))
#define DEVI __device__ __forceinline__

constexpr int S_ = 2048, D_ = 512, H_ = 32, E_ = 128, NSEG = 64, F_ = 1792, HE = 4096;
constexpr unsigned SENT = 0xFF81FF81u;
constexpr float SM_C = 0.08838834764831845f * 1.4426950408889634f;

constexpr size_t MB = 1u << 20;
constexpr size_t WS_EXCH = 0;
constexpr size_t WS_MOD = 2 * MB;
constexpr size_t WS_HF = 3 * MB;
constexpr size_t WS_KV = 5 * MB;
constexpr size_t WS_QM = 37 * MB;
constexpr size_t WS_PP = 69 * MB;
constexpr size_t WS_X1 = 101 * MB;
constexpr size_t WS_H2 = 103 * MB;
constexpr size_t WS_UF = 105 * MB;
constexpr size_t WS_DBG = 107 * MB;
constexpr size_t WS_END = 108 * MB;
constexpr size_t WS_CFG = WS_MOD + 16384;
constexpr size_t WS_SYNC = WS_MOD + 32768;
constexpr size_t SY_POL = 24576 + 8192 + 2048, SY_CD = 24576, SY_CL = 36864, SY_H = 24576 + 8192;
constexpr size_t SY_U = SY_H + 1024;
constexpr size_t SY_CD_F = 53248, SY_CL_C = SY_CD + 4096, SY_H_F = 69632, SY_HINT = 73728, SY_BYTES = 77824;
static_assert(WS_END == (108u << 20) && WS_SYNC + SY_POL == (2u << 20) + 32768 + 24576 + 8192 + 2048 && WS_KV == (5u << 20) && WS_QM == (37u << 20), "literals in the asm block");

DEVI float bf2f(u16 v) { return __uint_as_float((unsigned)v << 16); }
DEVI float bflo(unsigned v) { return __uint_as_float(v << 16); }
DEVI float bfhi(unsigned v) { return __uint_as_float(v & 0xffff0000u); }
DEVI unsigned pk2(float a, float b) { f32x2 f = {a, b}; return __builtin_bit_cast(unsigned, __builtin_convertvector(f, bf16x2)); }
DEVI float rbf(float a) { return bflo(pk2(a, 0.f)); }
DEVI f32x4 mfma32(u32x4 a, u32x4 b, f32x4 c) { return __builtin_amdgcn_mfma_f32_16x16x32_bf16(__builtin_bit_cast(bf16x8, a), __builtin_bit_cast(bf16x8, b), c, 0, 0, 0); }
DEVI f32x4 mfma16(u32x2 a, u32x2 b, f32x4 c) { return __builtin_amdgcn_mfma_f32_16x16x16bf16_1k(__builtin_bit_cast(s16x4, a), __builtin_bit_cast(s16x4, b), c, 0, 0, 0); }
DEVI f32x4 smf(u32x4 a, u32x8 b, f32x4 c, int idx) { return __builtin_amdgcn_smfmac_f32_16x16x64_bf16(__builtin_bit_cast(bf16x8, a), __builtin_bit_cast(bf16x16, b), c, idx, 0, 0); }
DEVI u32x2 trread(unsigned lds_addr) { return __builtin_bit_cast(u32x2, __builtin_amdgcn_ds_read_tr16_b64_v4i16((LAS s16x4*)lds_addr)); }
DEVI u32x4 ldsr128(unsigned a) { return *(LAS u32x4*)a; }
DEVI void ldsw128(unsigned a, u32x4 v) { *(LAS u32x4*)a = v; }
DEVI void ldsw64(unsigned a, u32x2 v) { *(LAS u32x2*)a = v; }
DEVI void glds16(const void* g, unsigned lds_uniform) {
  unsigned sv;
  asm volatile("s_mov_b32 %0, m0\n\ts_mov_b32 m0, %2\n\ts_nop 0\n\tglobal_load_lds_dwordx4 %1, off\n\ts_mov_b32 m0, %0" : "=&s"(sv) : "v"(g), "s"(lds_uniform) : "memory");
}
#define RAW_BARRIER() do { asm volatile("s_waitcnt lgkmcnt(0)" ::: "memory"); __builtin_amdgcn_s_barrier(); asm volatile("" ::: "memory"); } while (0)
template <int N> DEVI void wait_vm() { asm volatile("s_waitcnt vmcnt(%0)" :: "n"(N) : "memory"); }
DEVI float xmax32(float v) { auto r = __builtin_amdgcn_permlane32_swap(__float_as_uint(v), __float_as_uint(v), false, false); return fmaxf(__uint_as_float(r[0]), __uint_as_float(r[1])); }
DEVI float xmax16(float v) { auto r = __builtin_amdgcn_permlane16_swap(__float_as_uint(v), __float_as_uint(v), false, false); return fmaxf(__uint_as_float(r[0]), __uint_as_float(r[1])); }
DEVI float xsum32(float v) { auto r = __builtin_amdgcn_permlane32_swap(__float_as_uint(v), __float_as_uint(v), false, false); return __uint_as_float(r[0]) + __uint_as_float(r[1]); }
DEVI float xsum16(float v) { auto r = __builtin_amdgcn_permlane16_swap(__float_as_uint(v), __float_as_uint(v), false, false); return __uint_as_float(r[0]) + __uint_as_float(r[1]); }
template <int CTRL> DEVI unsigned dppmov(unsigned v) { return (unsigned)__builtin_amdgcn_update_dpp(0, (int)v, CTRL, 0xf, 0xf, true); }
template <int CTRL> DEVI float dppf(float v) { return __uint_as_float(dppmov<CTRL>(__float_as_uint(v))); }
DEVI float wave_sum(float v) {
  v += dppf<0x128>(v); v += dppf<0x124>(v); v += dppf<0x122>(v); v += dppf<0x121>(v);
  v = xsum16(v); v = xsum32(v); return v;
}

__global__ void __launch_bounds__(256) k_mod(const u16* __restrict__ c, const u16* __restrict__ wmod, const u16* __restrict__ mem0, unsigned char* __restrict__ ws,
                                             const u16* __restrict__ x, const u16* __restrict__ Wq, const u16* __restrict__ Wk, const u16* __restrict__ Wv, const u16* __restrict__ Wo) {
  const int tid = threadIdx.x;
  if (blockIdx.x >= 160) {
    const int pb = blockIdx.x - 160;
    const int which = pb < 64 ? 0 : 1 + ((pb - 64) >> 7), ib = pb < 64 ? pb : (pb - 64) & 127;
    const unsigned* src = (const unsigned*)(which == 0 ? x : which == 1 ? Wq : which == 2 ? Wk : which == 3 ? Wv : Wo) + ib * 8192 + tid * 32;
    const unsigned sv = *src;
    asm volatile("" :: "v"(sv));
    return;
  }
  if (blockIdx.x < 96) {
    __shared__ float red[64][33];
    const int cq = tid & 3, kq = tid >> 2, n0 = blockIdx.x * 32 + cq * 8;
    float acc[8] = {0, 0, 0, 0, 0, 0, 0, 0};
    u32x4 w[8];
#pragma unroll
    for (int i = 0; i < 8; ++i) w[i] = *(const u32x4*)(wmod + (size_t)(kq + 64 * i) * 3072 + n0);
#pragma unroll
    for (int i = 0; i < 8; ++i) {
      float cv = bf2f(c[kq + 64 * i]);
      float sv = rbf(cv / (1.f + __expf(-cv)));
#pragma unroll
      for (int j = 0; j < 4; ++j) { acc[2 * j] += sv * bflo(w[i][j]); acc[2 * j + 1] += sv * bfhi(w[i][j]); }
    }
#pragma unroll
    for (int j = 0; j < 8; ++j) red[kq][cq * 8 + j] = acc[j];
    __syncthreads();
    if (tid < 32) {
      float s = 0;
      for (int k = 0; k < 64; ++k) s += red[k][tid];
      ((float*)(ws + WS_MOD))[blockIdx.x * 32 + tid] = rbf(s);
    }
  } else {
    const int b = blockIdx.x - 96;
    u32x4 sv = {SENT, SENT, SENT, SENT};
    if (b == 0 && tid == 0) *(unsigned*)(ws + WS_CFG) = (unsigned)__builtin_amdgcn_s_getreg(20 | (3 << 11)) & 7u;
    static_assert(SY_BYTES == 64 * 76 * 16, "sync block");
    if (tid < 76) *(u32x4*)(ws + WS_SYNC + (b * 76 + tid) * 16) = (u32x4){0, 0, 0, 0};
    for (int i = tid; i < 2048; i += 256) {
      const unsigned off = b * 32768 + i * 16;
      u32x4 v = sv;
      if (off >= MB && off < MB + 16384) {
        const unsigned o = (off - MB) & 8191;
        const int row = (o >> 4) & 7, dp0 = (o >> 7) * 8;
        u16 e[8];
#pragma unroll
        for (int j = 0; j < 8; ++j) { const int dp = dp0 + j, d = (dp & ~63) + 32 * (dp & 1) + ((dp & 63) >> 1); e[j] = mem0[row * 512 + d]; }
#pragma unroll
        for (int j = 0; j < 4; ++j) { unsigned x = e[2 * j] | ((unsigned)e[2 * j + 1] << 16); v[j] = (x == SENT) ? 0xFFC1FFC1u : x; }
      }
      *(u32x4*)(ws + WS_EXCH + off) = v;
    }
  }
}

#define TSTAMP(i)
#define TSTAMP2(i)
template <bool SC1> DEVI void gldsS(unsigned voff, const void* sbase, unsigned lds_uniform) {
  unsigned sv;
  if (SC1) asm volatile("s_mov_b32 %0, m0\n\ts_mov_b32 m0, %3\n\ts_nop 0\n\tglobal_load_lds_dwordx4 %1, %2 sc1\n\ts_mov_b32 m0, %0" : "=&s"(sv) : "v"(voff), "s"(sbase), "s"(lds_uniform) : "memory");
  else asm volatile("s_mov_b32 %0, m0\n\ts_mov_b32 m0, %3\n\ts_nop 0\n\tglobal_load_lds_dwordx4 %1, %2\n\ts_mov_b32 m0, %0" : "=&s"(sv) : "v"(voff), "s"(sbase), "s"(lds_uniform) : "memory");
}
#define SCHED_FENCE() __builtin_amdgcn_sched_barrier(0)
#define MFMA_DRAIN() do { SCHED_FENCE(); asm volatile("s_nop 7\n\ts_nop 1" ::: "memory"); SCHED_FENCE(); } while (0)
template <bool B> struct BoolC { static constexpr bool value = B; };
template <int MT, int BN, int KS, int WM, int WN, bool WROW, int NSTG, bool XSC1>
struct Gemm {
  static constexpr int NWV = WM * WN, MW = MT / WM, NW = BN / 16 / WN;
  static constexpr int XB = MT * KS * 1024, WB = KS * 32 * BN * 2, SB = XB + WB;
  static constexpr int XI = XB / 1024 / NWV, WI = WB / 1024 / NWV, IPW = XI + WI;
  static_assert(XI * NWV * 1024 == XB && WI * NWV * 1024 == WB, "staging must split evenly over the waves");
  static_assert(KS % 2 == 0 && NW % 2 == 0 && NSTG >= 2 && NSTG * IPW < 64, "shape");
  static constexpr int LDS_BYTES = NSTG * SB;
  static constexpr int NMF = MW * NW, NRD = MW + 2 * NW;
  template <class PRE>
  static DEVI void run(f32x4 (&acc)[MW][NW], const unsigned char* X, int xks, int mt0, int ks0,
                       const u16* W, int ldw, int n0, int nstage, unsigned lds0, PRE pre) {
    const int tid = threadIdx.x, w = __builtin_amdgcn_readfirstlane(tid >> 6), l = tid & 63;
    const int wm = w / WN, wn = w % WN, g = l >> 4, q = (l >> 2) & 3, p = l & 3;
    unsigned vo[IPW];
#pragma unroll
    for (int i = 0; i < XI; ++i) { const int ii = w * XI + i, m = ii / KS, k = ii % KS; vo[i] = (m * xks + k) * 1024 + l * 16; }
#pragma unroll
    for (int i = 0; i < WI; ++i) {
      const int u = (w * WI + i) * 4 + g, kq = u / (BN / 32), nb = u % (BN / 32);
      vo[XI + i] = ((kq * 4 + q) * ldw + nb * 32 + (((p >> 1) ^ (kq & 1)) * 16) + (p & 1) * 8) * 2;
    }
    const unsigned char* xs = X + ((size_t)mt0 * xks + ks0) * 1024;
    const unsigned char* wsrc = (const unsigned char*)(W + (size_t)ks0 * 32 * ldw + n0);
    const int wstep = KS * 64 * ldw;
    const unsigned xdst = lds0 + w * XI * 1024, wdst = lds0 + XB + w * WI * 1024;
    auto ld1 = [&](int i, unsigned bo) {
      if (i < XI) gldsS<XSC1>(vo[i], xs, xdst + bo + i * 1024); else gldsS<false>(vo[i], wsrc, wdst + bo + (i - XI) * 1024);
    };
    const unsigned xrd = lds0 + (wm * MW) * KS * 1024 + l * 16;
    const unsigned wrd0 = lds0 + XB + g * (BN / 32) * 256 + q * 64 + p * 8 + (wn * NW / 2) * 256;
    const unsigned wrdE = wrd0 + (g & 1) * 32, wrdO = wrd0 + ((g & 1) ^ 1) * 32;
    u32x4 xf[2][MW]; u32x2 wf[2][NW][2];
    unsigned ax, aE, aO;
    auto rd1 = [&](int set, int k, int r) {
      if (r < MW) xf[set][r] = ldsr128(ax + (r * KS + k) * 1024);
      else { const int n = (r - MW) >> 1, hf = (r - MW) & 1; wf[set][n][hf] = trread(((n & 1) ? aO : aE) + ((k * 8 + hf * 4) * (BN / 32) + (n >> 1)) * 256); }
    };
#pragma unroll
    for (int s = 0; s < NSTG; ++s) {
#pragma unroll
      for (int i = XI; i < IPW; ++i) ld1(i, s * SB);
      wsrc += wstep;
    }
    pre();
#pragma unroll
    for (int s = 0; s < NSTG; ++s) {
#pragma unroll
      for (int i = 0; i < XI; ++i) ld1(i, s * SB);
      xs += KS * 1024;
    }
    wait_vm<0>();
    RAW_BARRIER();
    ax = xrd; aE = wrdE; aO = wrdO;
#pragma unroll
    for (int r = 0; r < NRD; ++r) rd1(0, 0, r);
    unsigned bo = 0;
    auto body = [&](auto more_c, int left) {
      constexpr bool more = decltype(more_c)::value;
#pragma unroll
      for (int k = 0; k < KS; ++k) {
        const int cur = k & 1;
        const bool last = k == KS - 1;
        if (last) {
          if (more || left >= NSTG - 2) wait_vm<(NSTG - 2) * IPW>();
          else if (NSTG > 3 && left >= 1) wait_vm<IPW>();
          else wait_vm<0>();
          RAW_BARRIER();
          const unsigned bn = (bo == (NSTG - 1) * SB) ? 0 : bo + SB;
          ax = xrd + bn; aE = wrdE + bn; aO = wrdO + bn;
        }
        SCHED_FENCE();
#pragma unroll
        for (int i = 0; i < NMF; ++i) {
          const int m = i / NW, n = i % NW;
          const u32x4 wv = {wf[cur][n][0][0], wf[cur][n][0][1], wf[cur][n][1][0], wf[cur][n][1][1]};
          acc[m][n] = WROW ? mfma32(wv, xf[cur][m], acc[m][n]) : mfma32(xf[cur][m], wv, acc[m][n]);
#pragma unroll
          for (int r = (i * NRD) / NMF; r < ((i + 1) * NRD) / NMF; ++r) rd1(cur ^ 1, last ? 0 : k + 1, r);
          if (last && more) {
#pragma unroll
            for (int j = (i * IPW) / NMF; j < ((i + 1) * IPW) / NMF; ++j) ld1(j, bo);
          }
          SCHED_FENCE();
        }
      }
      xs += KS * 1024; wsrc += wstep;
      bo = (bo == (NSTG - 1) * SB) ? 0 : bo + SB;
    };
#pragma unroll 1
    for (int t = 0; t < nstage - NSTG; ++t) body(BoolC<true>{}, 0);
#pragma unroll 1
    for (int t = 0; t < NSTG; ++t) body(BoolC<false>{}, NSTG - 2 - t);
    MFMA_DRAIN();
    RAW_BARRIER();
  }
};

constexpr int RL_MEM = 131072, RL_K = 139264, RL_Q = 147456, RL_KM = 149760, RL_O = 152064, RL_SEL = 156672, RL_END = 163840;

DEVI void rec_body(const u16* Wq, const u16* Wk, const u16* Wv, const u16* Wo, unsigned char* ws, int h, int pd1, int pd2) {
  const int tid = threadIdx.x, w = __builtin_amdgcn_readfirstlane(tid >> 6), l = tid & 63, c = l & 15, g = l >> 4, q4 = (l >> 2) & 3, p4 = l & 3, y = q4 >> 1;
  unsigned tab[25];
  tab[0] = c < 8 ? 0x4444 : 0xEEEE;
  tab[1] = w * 2048 + l * 16;
  tab[2] = (8 * w + 2 * (g >> 1) + (c >> 3)) * 16384 + (2 * ((c >> 2) & 1) + (g & 1)) * 2048 + h * 64 + (c & 3) * 16;
  tab[3] = l < 32 ? l * 256 : (l < 34 ? (unsigned)(WS_SYNC - MB) + ((l - 32) * 32 + h) * 256 : 0x7FFFFFF0u);
  tab[4] = (c < 4) ? (4 * h + w) * 64 + g * 16 + c * 4 : 0x7FFFFFF0u;
  tab[5] = 65536 + w * 8192 + l * 16;
  tab[6] = tab[7] = tab[8] = tab[9] = 0;
  tab[11] = RL_MEM + l * 64 + ((l >> 1) & 15) * 4;
  tab[10] = RL_MEM + (l ^ 8) * 16;
  tab[12] = RL_MEM + l * 16;
  tab[13] = w * 2048 + l * 8;
  tab[14] = RL_Q + (32 * w + 16 * (g >> 1) + c) * 8 + (g & 1) * 1088;
  tab[15] = RL_Q + (4 * g + q4) * 8 + (p4 & 1) * 1088;
  tab[16] = RL_O + (w >> 1) * 1024 + (32 * (w & 1) + 16 * (c >> 3) + 8 * (g >> 1) + (c & 7)) * 16 + (g & 1) * 8;
  tab[17] = w * 8192 + l * 16;
#pragma unroll
  for (int var = 0; var < 2; ++var) {
    tab[18 + var] = (32 * (q4 & 1) + 2 * (g & 1) + 4 * (var ^ y) + 8 * (g >> 1)) * 256 + (((2 * w + y) ^ (2 * (q4 & 1) + 4 * (g & 1))) * 32) + p4 * 8;
    tab[20 + var] = w * 8192 + (16 * (g >> 1) + 8 * (var ^ y) + 4 * (g & 1) + (q4 & 1)) * 256 + y * 32 + p4 * 8;
  }
  tab[22] = (32 * w + g) * 8192 + ((c ^ (4 * (w & 1) + 8 * (g >> 1))) * 16);
  tab[23] = g * 1024 + c * 16;
  tab[24] = (32 * (q4 & 1) + 2 * (g & 1) + 4 * y + 8 * (g >> 1)) * 256 + (((2 * w) ^ (2 * (q4 & 1) + 4 * (g & 1))) * 32) + p4 * 8;
#pragma unroll
  for (int i = 0; i < 25; ++i) *(LAS unsigned*)(RL_MEM + i * 1024 + tid * 4) = tab[i];
  {
    const int e = 2 * (c >> 2) + (c & 1);
    const bool used = (c & 3) < 2;
    u32x4 sel;
#pragma unroll
    for (int j = 0; j < 4; ++j) sel[j] = (used && (j & 1) == (e >> 2)) ? 0x3F80u : 0u;
    if (w == 0) { ldsw128(RL_SEL + l * 16, sel); *(LAS unsigned*)(RL_SEL + 1024 + l * 16) = (e & 3) * 0x5555u; }
  }
  const unsigned char* pq = (const unsigned char*)Wq + h * 256;
  const unsigned char* pk = (const unsigned char*)Wk + h * 256;
  const unsigned char* pv = (const unsigned char*)Wv + h * 256;
  const unsigned char* po = (const unsigned char*)Wo + h * 131072 + w * 256;
  asm volatile(
  "s_mov_b64 s[36:37], %0\n"
  "s_mov_b32 s38, 113246208\n"
  "s_mov_b32 s39, 0x00020000\n"
  "s_mov_b64 s[40:41], %1\n"
  "s_mov_b64 s[42:43], %2\n"
  "s_mov_b64 s[44:45], %3\n"
  "s_mov_b64 s[46:47], %4\n"
  "s_mov_b32 s48, %5\n"
  "s_mov_b32 s49, 2164736\n"
  "s_mov_b32 s50, %6\n"
  "s_mov_b32 s51, %7\n"
  "s_and_b32 s37, s37, 0xffff\n"
  "s_mov_b32 s52, 0xFF81FF81\n"
  "s_mov_b32 s60, 0x3e0293ee\n"
  "s_mov_b32 s63, 0\n"
  "s_mov_b32 s64, 0\n"
  "s_mov_b32 s99, 0\n"
  "v_mbcnt_lo_u32_b32 v24, -1, 0\n"
  "v_mbcnt_hi_u32_b32 v24, -1, v24\n"
  "s_lshl_b32 s58, s48, 8\n"
  "s_add_u32 s58, s58, 131072\n"
  "v_lshlrev_b32 v24, 2, v24\n"
  "v_add_u32 v24, s58, v24\n"
  "s_waitcnt lgkmcnt(0)\n"
  "ds_read_b32 v0, v24 offset:0\n"
  "ds_read_b32 v1, v24 offset:1024\n"
  "ds_read_b32 v2, v24 offset:2048\n"
  "ds_read_b32 v3, v24 offset:3072\n"
  "ds_read_b32 v4, v24 offset:4096\n"
  "ds_read_b32 v5, v24 offset:5120\n"
  "ds_read_b32 v6, v24 offset:6144\n"
  "ds_read_b32 v7, v24 offset:7168\n"
  "s_waitcnt lgkmcnt(0)\n"
  "ds_read_b32 v8, v24 offset:8192\n"
  "ds_read_b32 v9, v24 offset:9216\n"
  "ds_read_b32 v10, v24 offset:10240\n"
  "ds_read_b32 v11, v24 offset:11264\n"
  "ds_read_b32 v12, v24 offset:12288\n"
  "ds_read_b32 v13, v24 offset:13312\n"
  "ds_read_b32 v14, v24 offset:14336\n"
  "ds_read_b32 v15, v24 offset:15360\n"
  "s_waitcnt lgkmcnt(0)\n"
  "ds_read_b32 v16, v24 offset:16384\n"
  "ds_read_b32 v17, v24 offset:17408\n"
  "ds_read_b32 v18, v24 offset:18432\n"
  "ds_read_b32 v19, v24 offset:19456\n"
  "ds_read_b32 v20, v24 offset:20480\n"
  "ds_read_b32 v21, v24 offset:21504\n"
  "ds_read_b32 v22, v24 offset:22528\n"
  "ds_read_b32 v23, v24 offset:23552\n"
  "s_waitcnt lgkmcnt(0)\n"
  "ds_read_b32 v120, v24 offset:24576\n"
  "s_waitcnt lgkmcnt(0)\n"
  "s_lshl_b32 s58, s48, 13\n"
  "s_add_u32 m0, s58, 0\n"
  "s_add_u32 s96, s40, 0\n"
  "s_addc_u32 s97, s41, 0\n"
  "global_load_lds_dwordx4 v22, s[96:97]\n"
  "s_add_u32 s96, s96, 32768\n"
  "s_addc_u32 s97, s97, 0\n"
  "s_add_u32 m0, m0, 1024\n"
  "s_nop 0\n"
  "global_load_lds_dwordx4 v22, s[96:97]\n"
  "s_add_u32 s96, s96, 32768\n"
  "s_addc_u32 s97, s97, 0\n"
  "s_add_u32 m0, m0, 1024\n"
  "s_nop 0\n"
  "global_load_lds_dwordx4 v22, s[96:97]\n"
  "s_add_u32 s96, s96, 32768\n"
  "s_addc_u32 s97, s97, 0\n"
  "s_add_u32 m0, m0, 1024\n"
  "s_nop 0\n"
  "global_load_lds_dwordx4 v22, s[96:97]\n"
  "s_add_u32 s96, s96, 32768\n"
  "s_addc_u32 s97, s97, 0\n"
  "s_add_u32 m0, m0, 1024\n"
  "s_nop 0\n"
  "global_load_lds_dwordx4 v22, s[96:97]\n"
  "s_add_u32 s96, s96, 32768\n"
  "s_addc_u32 s97, s97, 0\n"
  "s_add_u32 m0, m0, 1024\n"
  "s_nop 0\n"
  "global_load_lds_dwordx4 v22, s[96:97]\n"
  "s_add_u32 s96, s96, 32768\n"
  "s_addc_u32 s97, s97, 0\n"
  "s_add_u32 m0, m0, 1024\n"
  "s_nop 0\n"
  "global_load_lds_dwordx4 v22, s[96:97]\n"
  "s_add_u32 s96, s96, 32768\n"
  "s_addc_u32 s97, s97, 0\n"
  "s_add_u32 m0, m0, 1024\n"
  "s_nop 0\n"
  "global_load_lds_dwordx4 v22, s[96:97]\n"
  "s_lshl_b32 s58, s48, 13\n"
  "s_add_u32 m0, s58, 32768\n"
  "s_add_u32 s96, s40, 1048576\n"
  "s_addc_u32 s97, s41, 0\n"
  "global_load_lds_dwordx4 v22, s[96:97]\n"
  "s_add_u32 s96, s96, 32768\n"
  "s_addc_u32 s97, s97, 0\n"
  "s_add_u32 m0, m0, 1024\n"
  "s_nop 0\n"
  "global_load_lds_dwordx4 v22, s[96:97]\n"
  "s_add_u32 s96, s96, 32768\n"
  "s_addc_u32 s97, s97, 0\n"
  "s_add_u32 m0, m0, 1024\n"
  "s_nop 0\n"
  "global_load_lds_dwordx4 v22, s[96:97]\n"
  "s_add_u32 s96, s96, 32768\n"
  "s_addc_u32 s97, s97, 0\n"
  "s_add_u32 m0, m0, 1024\n"
  "s_nop 0\n"
  "global_load_lds_dwordx4 v22, s[96:97]\n"
  "s_add_u32 s96, s96, 32768\n"
  "s_addc_u32 s97, s97, 0\n"
  "s_add_u32 m0, m0, 1024\n"
  "s_nop 0\n"
  "global_load_lds_dwordx4 v22, s[96:97]\n"
  "s_add_u32 s96, s96, 32768\n"
  "s_addc_u32 s97, s97, 0\n"
  "s_add_u32 m0, m0, 1024\n"
  "s_nop 0\n"
  "global_load_lds_dwordx4 v22, s[96:97]\n"
  "s_add_u32 s96, s96, 32768\n"
  "s_addc_u32 s97, s97, 0\n"
  "s_add_u32 m0, m0, 1024\n"
  "s_nop 0\n"
  "global_load_lds_dwordx4 v22, s[96:97]\n"
  "s_add_u32 s96, s96, 32768\n"
  "s_addc_u32 s97, s97, 0\n"
  "s_add_u32 m0, m0, 1024\n"
  "s_nop 0\n"
  "global_load_lds_dwordx4 v22, s[96:97]\n"
  "s_lshl_b32 s58, s48, 13\n"
  "s_add_u32 m0, s58, 65536\n"
  "s_add_u32 s96, s40, 2097152\n"
  "s_addc_u32 s97, s41, 0\n"
  "global_load_lds_dwordx4 v22, s[96:97]\n"
  "s_add_u32 s96, s96, 32768\n"
  "s_addc_u32 s97, s97, 0\n"
  "s_add_u32 m0, m0, 1024\n"
  "s_nop 0\n"
  "global_load_lds_dwordx4 v22, s[96:97]\n"
  "s_add_u32 s96, s96, 32768\n"
  "s_addc_u32 s97, s97, 0\n"
  "s_add_u32 m0, m0, 1024\n"
  "s_nop 0\n"
  "global_load_lds_dwordx4 v22, s[96:97]\n"
  "s_add_u32 s96, s96, 32768\n"
  "s_addc_u32 s97, s97, 0\n"
  "s_add_u32 m0, m0, 1024\n"
  "s_nop 0\n"
  "global_load_lds_dwordx4 v22, s[96:97]\n"
  "s_add_u32 s96, s96, 32768\n"
  "s_addc_u32 s97, s97, 0\n"
  "s_add_u32 m0, m0, 1024\n"
  "s_nop 0\n"
  "global_load_lds_dwordx4 v22, s[96:97]\n"
  "s_add_u32 s96, s96, 32768\n"
  "s_addc_u32 s97, s97, 0\n"
  "s_add_u32 m0, m0, 1024\n"
  "s_nop 0\n"
  "global_load_lds_dwordx4 v22, s[96:97]\n"
  "s_add_u32 s96, s96, 32768\n"
  "s_addc_u32 s97, s97, 0\n"
  "s_add_u32 m0, m0, 1024\n"
  "s_nop 0\n"
  "global_load_lds_dwordx4 v22, s[96:97]\n"
  "s_add_u32 s96, s96, 32768\n"
  "s_addc_u32 s97, s97, 0\n"
  "s_add_u32 m0, m0, 1024\n"
  "s_nop 0\n"
  "global_load_lds_dwordx4 v22, s[96:97]\n"
  "s_lshl_b32 s58, s48, 13\n"
  "s_add_u32 m0, s58, 98304\n"
  "s_add_u32 s96, s40, 3145728\n"
  "s_addc_u32 s97, s41, 0\n"
  "global_load_lds_dwordx4 v22, s[96:97]\n"
  "s_add_u32 s96, s96, 32768\n"
  "s_addc_u32 s97, s97, 0\n"
  "s_add_u32 m0, m0, 1024\n"
  "s_nop 0\n"
  "global_load_lds_dwordx4 v22, s[96:97]\n"
  "s_add_u32 s96, s96, 32768\n"
  "s_addc_u32 s97, s97, 0\n"
  "s_add_u32 m0, m0, 1024\n"
  "s_nop 0\n"
  "global_load_lds_dwordx4 v22, s[96:97]\n"
  "s_add_u32 s96, s96, 32768\n"
  "s_addc_u32 s97, s97, 0\n"
  "s_add_u32 m0, m0, 1024\n"
  "s_nop 0\n"
  "global_load_lds_dwordx4 v22, s[96:97]\n"
  "s_add_u32 s96, s96, 32768\n"
  "s_addc_u32 s97, s97, 0\n"
  "s_add_u32 m0, m0, 1024\n"
  "s_nop 0\n"
  "global_load_lds_dwordx4 v22, s[96:97]\n"
  "s_add_u32 s96, s96, 32768\n"
  "s_addc_u32 s97, s97, 0\n"
  "s_add_u32 m0, m0, 1024\n"
  "s_nop 0\n"
  "global_load_lds_dwordx4 v22, s[96:97]\n"
  "s_add_u32 s96, s96, 32768\n"
  "s_addc_u32 s97, s97, 0\n"
  "s_add_u32 m0, m0, 1024\n"
  "s_nop 0\n"
  "global_load_lds_dwordx4 v22, s[96:97]\n"
  "s_add_u32 s96, s96, 32768\n"
  "s_addc_u32 s97, s97, 0\n"
  "s_add_u32 m0, m0, 1024\n"
  "s_nop 0\n"
  "global_load_lds_dwordx4 v22, s[96:97]\n"
  "v_add_u32 v121, 0x10000, v18\n"
  "v_add_u32 v122, 0x10000, v19\n"
  "v_add_u32 v123, 0x10000, v120\n"
  "v_add_u32 v124, 0x10000, v20\n"
  "v_add_u32 v125, 0x10000, v21\n"
  "s_waitcnt vmcnt(24)\n"
  "s_barrier\n"
  "ds_read_b64_tr_b16 v[24:25], v18 offset:0\n"
  "ds_read_b64_tr_b16 v[26:27], v18 offset:256\n"
  "ds_read_b64_tr_b16 v[28:29], v18 offset:4096\n"
  "ds_read_b64_tr_b16 v[30:31], v18 offset:4352\n"
  "ds_read_b64_tr_b16 a[8:9], v19 offset:0\n"
  "ds_read_b64_tr_b16 a[10:11], v19 offset:256\n"
  "ds_read_b64_tr_b16 a[12:13], v19 offset:4096\n"
  "ds_read_b64_tr_b16 a[14:15], v19 offset:4352\n"
  "s_waitcnt lgkmcnt(0)\n"
  "ds_read_b64_tr_b16 v[40:41], v18 offset:16384\n"
  "ds_read_b64_tr_b16 v[42:43], v18 offset:16640\n"
  "ds_read_b64_tr_b16 v[44:45], v18 offset:20480\n"
  "ds_read_b64_tr_b16 v[46:47], v18 offset:20736\n"
  "ds_read_b64_tr_b16 a[24:25], v19 offset:16384\n"
  "ds_read_b64_tr_b16 a[26:27], v19 offset:16640\n"
  "ds_read_b64_tr_b16 a[28:29], v19 offset:20480\n"
  "ds_read_b64_tr_b16 a[30:31], v19 offset:20736\n"
  "s_waitcnt lgkmcnt(0)\n"
  "s_barrier\n"
  "s_lshl_b32 s58, s48, 13\n"
  "s_add_u32 m0, s58, 0\n"
  "s_add_u32 s96, s42, 0\n"
  "s_addc_u32 s97, s43, 0\n"
  "global_load_lds_dwordx4 v22, s[96:97]\n"
  "s_add_u32 s96, s96, 32768\n"
  "s_addc_u32 s97, s97, 0\n"
  "s_add_u32 m0, m0, 1024\n"
  "s_nop 0\n"
  "global_load_lds_dwordx4 v22, s[96:97]\n"
  "s_add_u32 s96, s96, 32768\n"
  "s_addc_u32 s97, s97, 0\n"
  "s_add_u32 m0, m0, 1024\n"
  "s_nop 0\n"
  "global_load_lds_dwordx4 v22, s[96:97]\n"
  "s_add_u32 s96, s96, 32768\n"
  "s_addc_u32 s97, s97, 0\n"
  "s_add_u32 m0, m0, 1024\n"
  "s_nop 0\n"
  "global_load_lds_dwordx4 v22, s[96:97]\n"
  "s_add_u32 s96, s96, 32768\n"
  "s_addc_u32 s97, s97, 0\n"
  "s_add_u32 m0, m0, 1024\n"
  "s_nop 0\n"
  "global_load_lds_dwordx4 v22, s[96:97]\n"
  "s_add_u32 s96, s96, 32768\n"
  "s_addc_u32 s97, s97, 0\n"
  "s_add_u32 m0, m0, 1024\n"
  "s_nop 0\n"
  "global_load_lds_dwordx4 v22, s[96:97]\n"
  "s_add_u32 s96, s96, 32768\n"
  "s_addc_u32 s97, s97, 0\n"
  "s_add_u32 m0, m0, 1024\n"
  "s_nop 0\n"
  "global_load_lds_dwordx4 v22, s[96:97]\n"
  "s_add_u32 s96, s96, 32768\n"
  "s_addc_u32 s97, s97, 0\n"
  "s_add_u32 m0, m0, 1024\n"
  "s_nop 0\n"
  "global_load_lds_dwordx4 v22, s[96:97]\n"
  "s_waitcnt vmcnt(24)\n"
  "s_barrier\n"
  "ds_read_b64_tr_b16 v[56:57], v18 offset:32768\n"
  "ds_read_b64_tr_b16 v[58:59], v18 offset:33024\n"
  "ds_read_b64_tr_b16 v[60:61], v18 offset:36864\n"
  "ds_read_b64_tr_b16 v[62:63], v18 offset:37120\n"
  "ds_read_b64_tr_b16 a[40:41], v19 offset:32768\n"
  "ds_read_b64_tr_b16 a[42:43], v19 offset:33024\n"
  "ds_read_b64_tr_b16 a[44:45], v19 offset:36864\n"
  "ds_read_b64_tr_b16 a[46:47], v19 offset:37120\n"
  "s_waitcnt lgkmcnt(0)\n"
  "ds_read_b64_tr_b16 v[72:73], v18 offset:49152\n"
  "ds_read_b64_tr_b16 v[74:75], v18 offset:49408\n"
  "ds_read_b64_tr_b16 v[76:77], v18 offset:53248\n"
  "ds_read_b64_tr_b16 v[78:79], v18 offset:53504\n"
  "ds_read_b64_tr_b16 a[56:57], v19 offset:49152\n"
  "ds_read_b64_tr_b16 a[58:59], v19 offset:49408\n"
  "ds_read_b64_tr_b16 a[60:61], v19 offset:53248\n"
  "ds_read_b64_tr_b16 a[62:63], v19 offset:53504\n"
  "s_waitcnt lgkmcnt(0)\n"
  "s_barrier\n"
  "s_lshl_b32 s58, s48, 13\n"
  "s_add_u32 m0, s58, 32768\n"
  "s_add_u32 s96, s42, 1048576\n"
  "s_addc_u32 s97, s43, 0\n"
  "global_load_lds_dwordx4 v22, s[96:97]\n"
  "s_add_u32 s96, s96, 32768\n"
  "s_addc_u32 s97, s97, 0\n"
  "s_add_u32 m0, m0, 1024\n"
  "s_nop 0\n"
  "global_load_lds_dwordx4 v22, s[96:97]\n"
  "s_add_u32 s96, s96, 32768\n"
  "s_addc_u32 s97, s97, 0\n"
  "s_add_u32 m0, m0, 1024\n"
  "s_nop 0\n"
  "global_load_lds_dwordx4 v22, s[96:97]\n"
  "s_add_u32 s96, s96, 32768\n"
  "s_addc_u32 s97, s97, 0\n"
  "s_add_u32 m0, m0, 1024\n"
  "s_nop 0\n"
  "global_load_lds_dwordx4 v22, s[96:97]\n"
  "s_add_u32 s96, s96, 32768\n"
  "s_addc_u32 s97, s97, 0\n"
  "s_add_u32 m0, m0, 1024\n"
  "s_nop 0\n"
  "global_load_lds_dwordx4 v22, s[96:97]\n"
  "s_add_u32 s96, s96, 32768\n"
  "s_addc_u32 s97, s97, 0\n"
  "s_add_u32 m0, m0, 1024\n"
  "s_nop 0\n"
  "global_load_lds_dwordx4 v22, s[96:97]\n"
  "s_add_u32 s96, s96, 32768\n"
  "s_addc_u32 s97, s97, 0\n"
  "s_add_u32 m0, m0, 1024\n"
  "s_nop 0\n"
  "global_load_lds_dwordx4 v22, s[96:97]\n"
  "s_add_u32 s96, s96, 32768\n"
  "s_addc_u32 s97, s97, 0\n"
  "s_add_u32 m0, m0, 1024\n"
  "s_nop 0\n"
  "global_load_lds_dwordx4 v22, s[96:97]\n"
  "s_waitcnt vmcnt(24)\n"
  "s_barrier\n"
  "ds_read_b64_tr_b16 a[64:65], v121 offset:0\n"
  "ds_read_b64_tr_b16 a[66:67], v121 offset:256\n"
  "ds_read_b64_tr_b16 a[68:69], v121 offset:4096\n"
  "ds_read_b64_tr_b16 a[70:71], v121 offset:4352\n"
  "ds_read_b64_tr_b16 a[72:73], v122 offset:0\n"
  "ds_read_b64_tr_b16 a[74:75], v122 offset:256\n"
  "ds_read_b64_tr_b16 a[76:77], v122 offset:4096\n"
  "ds_read_b64_tr_b16 a[78:79], v122 offset:4352\n"
  "s_waitcnt lgkmcnt(0)\n"
  "ds_read_b64_tr_b16 a[80:81], v121 offset:16384\n"
  "ds_read_b64_tr_b16 a[82:83], v121 offset:16640\n"
  "ds_read_b64_tr_b16 a[84:85], v121 offset:20480\n"
  "ds_read_b64_tr_b16 a[86:87], v121 offset:20736\n"
  "ds_read_b64_tr_b16 a[88:89], v122 offset:16384\n"
  "ds_read_b64_tr_b16 a[90:91], v122 offset:16640\n"
  "ds_read_b64_tr_b16 a[92:93], v122 offset:20480\n"
  "ds_read_b64_tr_b16 a[94:95], v122 offset:20736\n"
  "s_waitcnt lgkmcnt(0)\n"
  "s_barrier\n"
  "s_lshl_b32 s58, s48, 13\n"
  "s_add_u32 m0, s58, 65536\n"
  "s_add_u32 s96, s42, 2097152\n"
  "s_addc_u32 s97, s43, 0\n"
  "global_load_lds_dwordx4 v22, s[96:97]\n"
  "s_add_u32 s96, s96, 32768\n"
  "s_addc_u32 s97, s97, 0\n"
  "s_add_u32 m0, m0, 1024\n"
  "s_nop 0\n"
  "global_load_lds_dwordx4 v22, s[96:97]\n"
  "s_add_u32 s96, s96, 32768\n"
  "s_addc_u32 s97, s97, 0\n"
  "s_add_u32 m0, m0, 1024\n"
  "s_nop 0\n"
  "global_load_lds_dwordx4 v22, s[96:97]\n"
  "s_add_u32 s96, s96, 32768\n"
  "s_addc_u32 s97, s97, 0\n"
  "s_add_u32 m0, m0, 1024\n"
  "s_nop 0\n"
  "global_load_lds_dwordx4 v22, s[96:97]\n"
  "s_add_u32 s96, s96, 32768\n"
  "s_addc_u32 s97, s97, 0\n"
  "s_add_u32 m0, m0, 1024\n"
  "s_nop 0\n"
  "global_load_lds_dwordx4 v22, s[96:97]\n"
  "s_add_u32 s96, s96, 32768\n"
  "s_addc_u32 s97, s97, 0\n"
  "s_add_u32 m0, m0, 1024\n"
  "s_nop 0\n"
  "global_load_lds_dwordx4 v22, s[96:97]\n"
  "s_add_u32 s96, s96, 32768\n"
  "s_addc_u32 s97, s97, 0\n"
  "s_add_u32 m0, m0, 1024\n"
  "s_nop 0\n"
  "global_load_lds_dwordx4 v22, s[96:97]\n"
  "s_add_u32 s96, s96, 32768\n"
  "s_addc_u32 s97, s97, 0\n"
  "s_add_u32 m0, m0, 1024\n"
  "s_nop 0\n"
  "global_load_lds_dwordx4 v22, s[96:97]\n"
  "s_waitcnt vmcnt(24)\n"
  "s_barrier\n"
  "ds_read_b64_tr_b16 a[96:97], v121 offset:32768\n"
  "ds_read_b64_tr_b16 a[98:99], v121 offset:33024\n"
  "ds_read_b64_tr_b16 a[100:101], v121 offset:36864\n"
  "ds_read_b64_tr_b16 a[102:103], v121 offset:37120\n"
  "ds_read_b64_tr_b16 a[104:105], v122 offset:32768\n"
  "ds_read_b64_tr_b16 a[106:107], v122 offset:33024\n"
  "ds_read_b64_tr_b16 a[108:109], v122 offset:36864\n"
  "ds_read_b64_tr_b16 a[110:111], v122 offset:37120\n"
  "s_waitcnt lgkmcnt(0)\n"
  "ds_read_b64_tr_b16 a[112:113], v121 offset:49152\n"
  "ds_read_b64_tr_b16 a[114:115], v121 offset:49408\n"
  "ds_read_b64_tr_b16 a[116:117], v121 offset:53248\n"
  "ds_read_b64_tr_b16 a[118:119], v121 offset:53504\n"
  "ds_read_b64_tr_b16 a[120:121], v122 offset:49152\n"
  "ds_read_b64_tr_b16 a[122:123], v122 offset:49408\n"
  "ds_read_b64_tr_b16 a[124:125], v122 offset:53248\n"
  "ds_read_b64_tr_b16 a[126:127], v122 offset:53504\n"
  "s_waitcnt lgkmcnt(0)\n"
  "s_barrier\n"
  "s_lshl_b32 s58, s48, 13\n"
  "s_add_u32 m0, s58, 98304\n"
  "s_add_u32 s96, s42, 3145728\n"
  "s_addc_u32 s97, s43, 0\n"
  "global_load_lds_dwordx4 v22, s[96:97]\n"
  "s_add_u32 s96, s96, 32768\n"
  "s_addc_u32 s97, s97, 0\n"
  "s_add_u32 m0, m0, 1024\n"
  "s_nop 0\n"
  "global_load_lds_dwordx4 v22, s[96:97]\n"
  "s_add_u32 s96, s96, 32768\n"
  "s_addc_u32 s97, s97, 0\n"
  "s_add_u32 m0, m0, 1024\n"
  "s_nop 0\n"
  "global_load_lds_dwordx4 v22, s[96:97]\n"
  "s_add_u32 s96, s96, 32768\n"
  "s_addc_u32 s97, s97, 0\n"
  "s_add_u32 m0, m0, 1024\n"
  "s_nop 0\n"
  "global_load_lds_dwordx4 v22, s[96:97]\n"
  "s_add_u32 s96, s96, 32768\n"
  "s_addc_u32 s97, s97, 0\n"
  "s_add_u32 m0, m0, 1024\n"
  "s_nop 0\n"
  "global_load_lds_dwordx4 v22, s[96:97]\n"
  "s_add_u32 s96, s96, 32768\n"
  "s_addc_u32 s97, s97, 0\n"
  "s_add_u32 m0, m0, 1024\n"
  "s_nop 0\n"
  "global_load_lds_dwordx4 v22, s[96:97]\n"
  "s_add_u32 s96, s96, 32768\n"
  "s_addc_u32 s97, s97, 0\n"
  "s_add_u32 m0, m0, 1024\n"
  "s_nop 0\n"
  "global_load_lds_dwordx4 v22, s[96:97]\n"
  "s_add_u32 s96, s96, 32768\n"
  "s_addc_u32 s97, s97, 0\n"
  "s_add_u32 m0, m0, 1024\n"
  "s_nop 0\n"
  "global_load_lds_dwordx4 v22, s[96:97]\n"
  "s_waitcnt vmcnt(24)\n"
  "s_barrier\n"
  "ds_read_b64_tr_b16 v[32:33], v18 offset:0\n"
  "ds_read_b64_tr_b16 v[34:35], v18 offset:256\n"
  "ds_read_b64_tr_b16 v[36:37], v18 offset:4096\n"
  "ds_read_b64_tr_b16 v[38:39], v18 offset:4352\n"
  "ds_read_b64_tr_b16 a[136:137], v19 offset:0\n"
  "ds_read_b64_tr_b16 a[138:139], v19 offset:256\n"
  "ds_read_b64_tr_b16 a[140:141], v19 offset:4096\n"
  "ds_read_b64_tr_b16 a[142:143], v19 offset:4352\n"
  "s_waitcnt lgkmcnt(0)\n"
  "ds_read_b64_tr_b16 v[48:49], v18 offset:16384\n"
  "ds_read_b64_tr_b16 v[50:51], v18 offset:16640\n"
  "ds_read_b64_tr_b16 v[52:53], v18 offset:20480\n"
  "ds_read_b64_tr_b16 v[54:55], v18 offset:20736\n"
  "ds_read_b64_tr_b16 a[152:153], v19 offset:16384\n"
  "ds_read_b64_tr_b16 a[154:155], v19 offset:16640\n"
  "ds_read_b64_tr_b16 a[156:157], v19 offset:20480\n"
  "ds_read_b64_tr_b16 a[158:159], v19 offset:20736\n"
  "s_waitcnt lgkmcnt(0)\n"
  "s_barrier\n"
  "s_lshl_b32 s58, s48, 13\n"
  "s_add_u32 m0, s58, 0\n"
  "s_add_u32 s96, s44, 0\n"
  "s_addc_u32 s97, s45, 0\n"
  "global_load_lds_dwordx4 v22, s[96:97]\n"
  "s_add_u32 s96, s96, 32768\n"
  "s_addc_u32 s97, s97, 0\n"
  "s_add_u32 m0, m0, 1024\n"
  "s_nop 0\n"
  "global_load_lds_dwordx4 v22, s[96:97]\n"
  "s_add_u32 s96, s96, 32768\n"
  "s_addc_u32 s97, s97, 0\n"
  "s_add_u32 m0, m0, 1024\n"
  "s_nop 0\n"
  "global_load_lds_dwordx4 v22, s[96:97]\n"
  "s_add_u32 s96, s96, 32768\n"
  "s_addc_u32 s97, s97, 0\n"
  "s_add_u32 m0, m0, 1024\n"
  "s_nop 0\n"
  "global_load_lds_dwordx4 v22, s[96:97]\n"
  "s_add_u32 s96, s96, 32768\n"
  "s_addc_u32 s97, s97, 0\n"
  "s_add_u32 m0, m0, 1024\n"
  "s_nop 0\n"
  "global_load_lds_dwordx4 v22, s[96:97]\n"
  "s_add_u32 s96, s96, 32768\n"
  "s_addc_u32 s97, s97, 0\n"
  "s_add_u32 m0, m0, 1024\n"
  "s_nop 0\n"
  "global_load_lds_dwordx4 v22, s[96:97]\n"
  "s_add_u32 s96, s96, 32768\n"
  "s_addc_u32 s97, s97, 0\n"
  "s_add_u32 m0, m0, 1024\n"
  "s_nop 0\n"
  "global_load_lds_dwordx4 v22, s[96:97]\n"
  "s_add_u32 s96, s96, 32768\n"
  "s_addc_u32 s97, s97, 0\n"
  "s_add_u32 m0, m0, 1024\n"
  "s_nop 0\n"
  "global_load_lds_dwordx4 v22, s[96:97]\n"
  "s_waitcnt vmcnt(24)\n"
  "s_barrier\n"
  "ds_read_b64_tr_b16 v[64:65], v18 offset:32768\n"
  "ds_read_b64_tr_b16 v[66:67], v18 offset:33024\n"
  "ds_read_b64_tr_b16 v[68:69], v18 offset:36864\n"
  "ds_read_b64_tr_b16 v[70:71], v18 offset:37120\n"
  "ds_read_b64_tr_b16 a[168:169], v19 offset:32768\n"
  "ds_read_b64_tr_b16 a[170:171], v19 offset:33024\n"
  "ds_read_b64_tr_b16 a[172:173], v19 offset:36864\n"
  "ds_read_b64_tr_b16 a[174:175], v19 offset:37120\n"
  "s_waitcnt lgkmcnt(0)\n"
  "ds_read_b64_tr_b16 v[80:81], v18 offset:49152\n"
  "ds_read_b64_tr_b16 v[82:83], v18 offset:49408\n"
  "ds_read_b64_tr_b16 v[84:85], v18 offset:53248\n"
  "ds_read_b64_tr_b16 v[86:87], v18 offset:53504\n"
  "ds_read_b64_tr_b16 a[184:185], v19 offset:49152\n"
  "ds_read_b64_tr_b16 a[186:187], v19 offset:49408\n"
  "ds_read_b64_tr_b16 a[188:189], v19 offset:53248\n"
  "ds_read_b64_tr_b16 a[190:191], v19 offset:53504\n"
  "s_waitcnt lgkmcnt(0)\n"
  "s_barrier\n"
  "s_lshl_b32 s58, s48, 13\n"
  "s_add_u32 m0, s58, 32768\n"
  "s_add_u32 s96, s44, 1048576\n"
  "s_addc_u32 s97, s45, 0\n"
  "global_load_lds_dwordx4 v22, s[96:97]\n"
  "s_add_u32 s96, s96, 32768\n"
  "s_addc_u32 s97, s97, 0\n"
  "s_add_u32 m0, m0, 1024\n"
  "s_nop 0\n"
  "global_load_lds_dwordx4 v22, s[96:97]\n"
  "s_add_u32 s96, s96, 32768\n"
  "s_addc_u32 s97, s97, 0\n"
  "s_add_u32 m0, m0, 1024\n"
  "s_nop 0\n"
  "global_load_lds_dwordx4 v22, s[96:97]\n"
  "s_add_u32 s96, s96, 32768\n"
  "s_addc_u32 s97, s97, 0\n"
  "s_add_u32 m0, m0, 1024\n"
  "s_nop 0\n"
  "global_load_lds_dwordx4 v22, s[96:97]\n"
  "s_add_u32 s96, s96, 32768\n"
  "s_addc_u32 s97, s97, 0\n"
  "s_add_u32 m0, m0, 1024\n"
  "s_nop 0\n"
  "global_load_lds_dwordx4 v22, s[96:97]\n"
  "s_add_u32 s96, s96, 32768\n"
  "s_addc_u32 s97, s97, 0\n"
  "s_add_u32 m0, m0, 1024\n"
  "s_nop 0\n"
  "global_load_lds_dwordx4 v22, s[96:97]\n"
  "s_add_u32 s96, s96, 32768\n"
  "s_addc_u32 s97, s97, 0\n"
  "s_add_u32 m0, m0, 1024\n"
  "s_nop 0\n"
  "global_load_lds_dwordx4 v22, s[96:97]\n"
  "s_add_u32 s96, s96, 32768\n"
  "s_addc_u32 s97, s97, 0\n"
  "s_add_u32 m0, m0, 1024\n"
  "s_nop 0\n"
  "global_load_lds_dwordx4 v22, s[96:97]\n"
  "s_waitcnt vmcnt(24)\n"
  "s_barrier\n"
  "ds_read_b64_tr_b16 a[192:193], v121 offset:0\n"
  "ds_read_b64_tr_b16 a[194:195], v121 offset:256\n"
  "ds_read_b64_tr_b16 a[196:197], v121 offset:4096\n"
  "ds_read_b64_tr_b16 a[198:199], v121 offset:4352\n"
  "ds_read_b64_tr_b16 a[200:201], v122 offset:0\n"
  "ds_read_b64_tr_b16 a[202:203], v122 offset:256\n"
  "ds_read_b64_tr_b16 a[204:205], v122 offset:4096\n"
  "ds_read_b64_tr_b16 a[206:207], v122 offset:4352\n"
  "s_waitcnt lgkmcnt(0)\n"
  "ds_read_b64_tr_b16 a[208:209], v121 offset:16384\n"
  "ds_read_b64_tr_b16 a[210:211], v121 offset:16640\n"
  "ds_read_b64_tr_b16 a[212:213], v121 offset:20480\n"
  "ds_read_b64_tr_b16 a[214:215], v121 offset:20736\n"
  "ds_read_b64_tr_b16 a[216:217], v122 offset:16384\n"
  "ds_read_b64_tr_b16 a[218:219], v122 offset:16640\n"
  "ds_read_b64_tr_b16 a[220:221], v122 offset:20480\n"
  "ds_read_b64_tr_b16 a[222:223], v122 offset:20736\n"
  "s_waitcnt lgkmcnt(0)\n"
  "s_barrier\n"
  "s_lshl_b32 s58, s48, 13\n"
  "s_add_u32 m0, s58, 65536\n"
  "s_add_u32 s96, s44, 2097152\n"
  "s_addc_u32 s97, s45, 0\n"
  "global_load_lds_dwordx4 v22, s[96:97]\n"
  "s_add_u32 s96, s96, 32768\n"
  "s_addc_u32 s97, s97, 0\n"
  "s_add_u32 m0, m0, 1024\n"
  "s_nop 0\n"
  "global_load_lds_dwordx4 v22, s[96:97]\n"
  "s_add_u32 s96, s96, 32768\n"
  "s_addc_u32 s97, s97, 0\n"
  "s_add_u32 m0, m0, 1024\n"
  "s_nop 0\n"
  "global_load_lds_dwordx4 v22, s[96:97]\n"
  "s_add_u32 s96, s96, 32768\n"
  "s_addc_u32 s97, s97, 0\n"
  "s_add_u32 m0, m0, 1024\n"
  "s_nop 0\n"
  "global_load_lds_dwordx4 v22, s[96:97]\n"
  "s_add_u32 s96, s96, 32768\n"
  "s_addc_u32 s97, s97, 0\n"
  "s_add_u32 m0, m0, 1024\n"
  "s_nop 0\n"
  "global_load_lds_dwordx4 v22, s[96:97]\n"
  "s_add_u32 s96, s96, 32768\n"
  "s_addc_u32 s97, s97, 0\n"
  "s_add_u32 m0, m0, 1024\n"
  "s_nop 0\n"
  "global_load_lds_dwordx4 v22, s[96:97]\n"
  "s_add_u32 s96, s96, 32768\n"
  "s_addc_u32 s97, s97, 0\n"
  "s_add_u32 m0, m0, 1024\n"
  "s_nop 0\n"
  "global_load_lds_dwordx4 v22, s[96:97]\n"
  "s_add_u32 s96, s96, 32768\n"
  "s_addc_u32 s97, s97, 0\n"
  "s_add_u32 m0, m0, 1024\n"
  "s_nop 0\n"
  "global_load_lds_dwordx4 v22, s[96:97]\n"
  "s_waitcnt vmcnt(24)\n"
  "s_barrier\n"
  "ds_read_b64_tr_b16 a[224:225], v121 offset:32768\n"
  "ds_read_b64_tr_b16 a[226:227], v121 offset:33024\n"
  "ds_read_b64_tr_b16 a[228:229], v121 offset:36864\n"
  "ds_read_b64_tr_b16 a[230:231], v121 offset:37120\n"
  "ds_read_b64_tr_b16 a[232:233], v122 offset:32768\n"
  "ds_read_b64_tr_b16 a[234:235], v122 offset:33024\n"
  "ds_read_b64_tr_b16 a[236:237], v122 offset:36864\n"
  "ds_read_b64_tr_b16 a[238:239], v122 offset:37120\n"
  "s_waitcnt lgkmcnt(0)\n"
  "ds_read_b64_tr_b16 a[240:241], v121 offset:49152\n"
  "ds_read_b64_tr_b16 a[242:243], v121 offset:49408\n"
  "ds_read_b64_tr_b16 a[244:245], v121 offset:53248\n"
  "ds_read_b64_tr_b16 a[246:247], v121 offset:53504\n"
  "ds_read_b64_tr_b16 a[248:249], v122 offset:49152\n"
  "ds_read_b64_tr_b16 a[250:251], v122 offset:49408\n"
  "ds_read_b64_tr_b16 a[252:253], v122 offset:53248\n"
  "ds_read_b64_tr_b16 a[254:255], v122 offset:53504\n"
  "s_waitcnt lgkmcnt(0)\n"
  "s_barrier\n"
  "s_lshl_b32 s58, s48, 13\n"
  "s_add_u32 m0, s58, 98304\n"
  "s_add_u32 s96, s44, 3145728\n"
  "s_addc_u32 s97, s45, 0\n"
  "global_load_lds_dwordx4 v22, s[96:97]\n"
  "s_add_u32 s96, s96, 32768\n"
  "s_addc_u32 s97, s97, 0\n"
  "s_add_u32 m0, m0, 1024\n"
  "s_nop 0\n"
  "global_load_lds_dwordx4 v22, s[96:97]\n"
  "s_add_u32 s96, s96, 32768\n"
  "s_addc_u32 s97, s97, 0\n"
  "s_add_u32 m0, m0, 1024\n"
  "s_nop 0\n"
  "global_load_lds_dwordx4 v22, s[96:97]\n"
  "s_add_u32 s96, s96, 32768\n"
  "s_addc_u32 s97, s97, 0\n"
  "s_add_u32 m0, m0, 1024\n"
  "s_nop 0\n"
  "global_load_lds_dwordx4 v22, s[96:97]\n"
  "s_add_u32 s96, s96, 32768\n"
  "s_addc_u32 s97, s97, 0\n"
  "s_add_u32 m0, m0, 1024\n"
  "s_nop 0\n"
  "global_load_lds_dwordx4 v22, s[96:97]\n"
  "s_add_u32 s96, s96, 32768\n"
  "s_addc_u32 s97, s97, 0\n"
  "s_add_u32 m0, m0, 1024\n"
  "s_nop 0\n"
  "global_load_lds_dwordx4 v22, s[96:97]\n"
  "s_add_u32 s96, s96, 32768\n"
  "s_addc_u32 s97, s97, 0\n"
  "s_add_u32 m0, m0, 1024\n"
  "s_nop 0\n"
  "global_load_lds_dwordx4 v22, s[96:97]\n"
  "s_add_u32 s96, s96, 32768\n"
  "s_addc_u32 s97, s97, 0\n"
  "s_add_u32 m0, m0, 1024\n"
  "s_nop 0\n"
  "global_load_lds_dwordx4 v22, s[96:97]\n"
  "s_waitcnt vmcnt(24)\n"
  "s_barrier\n"
  "ds_read_b64_tr_b16 v[128:129], v120 offset:0\n"
  "ds_read_b64_tr_b16 v[130:131], v120 offset:256\n"
  "ds_read_b64_tr_b16 v[132:133], v120 offset:4096\n"
  "ds_read_b64_tr_b16 v[134:135], v120 offset:4352\n"
  "ds_read_b64_tr_b16 v[136:137], v120 offset:32\n"
  "ds_read_b64_tr_b16 v[138:139], v120 offset:288\n"
  "ds_read_b64_tr_b16 v[140:141], v120 offset:4128\n"
  "ds_read_b64_tr_b16 v[142:143], v120 offset:4384\n"
  "s_waitcnt lgkmcnt(0)\n"
  "ds_read_b64_tr_b16 v[144:145], v120 offset:16384\n"
  "ds_read_b64_tr_b16 v[146:147], v120 offset:16640\n"
  "ds_read_b64_tr_b16 v[148:149], v120 offset:20480\n"
  "ds_read_b64_tr_b16 v[150:151], v120 offset:20736\n"
  "ds_read_b64_tr_b16 v[152:153], v120 offset:16416\n"
  "ds_read_b64_tr_b16 v[154:155], v120 offset:16672\n"
  "ds_read_b64_tr_b16 v[156:157], v120 offset:20512\n"
  "ds_read_b64_tr_b16 v[158:159], v120 offset:20768\n"
  "s_waitcnt lgkmcnt(0)\n"
  "s_barrier\n"
  "s_lshl_b32 s58, s48, 13\n"
  "s_add_u32 m0, s58, 0\n"
  "s_add_u32 s96, s46, 0\n"
  "s_addc_u32 s97, s47, 0\n"
  "global_load_lds_dwordx4 v23, s[96:97]\n"
  "s_add_u32 s96, s96, 4096\n"
  "s_addc_u32 s97, s97, 0\n"
  "s_add_u32 m0, m0, 1024\n"
  "s_nop 0\n"
  "global_load_lds_dwordx4 v23, s[96:97]\n"
  "s_add_u32 s96, s96, 4096\n"
  "s_addc_u32 s97, s97, 0\n"
  "s_add_u32 m0, m0, 1024\n"
  "s_nop 0\n"
  "global_load_lds_dwordx4 v23, s[96:97]\n"
  "s_add_u32 s96, s96, 4096\n"
  "s_addc_u32 s97, s97, 0\n"
  "s_add_u32 m0, m0, 1024\n"
  "s_nop 0\n"
  "global_load_lds_dwordx4 v23, s[96:97]\n"
  "s_add_u32 s96, s96, 4096\n"
  "s_addc_u32 s97, s97, 0\n"
  "s_add_u32 m0, m0, 1024\n"
  "s_nop 0\n"
  "global_load_lds_dwordx4 v23, s[96:97]\n"
  "s_add_u32 s96, s96, 4096\n"
  "s_addc_u32 s97, s97, 0\n"
  "s_add_u32 m0, m0, 1024\n"
  "s_nop 0\n"
  "global_load_lds_dwordx4 v23, s[96:97]\n"
  "s_add_u32 s96, s96, 4096\n"
  "s_addc_u32 s97, s97, 0\n"
  "s_add_u32 m0, m0, 1024\n"
  "s_nop 0\n"
  "global_load_lds_dwordx4 v23, s[96:97]\n"
  "s_add_u32 s96, s96, 4096\n"
  "s_addc_u32 s97, s97, 0\n"
  "s_add_u32 m0, m0, 1024\n"
  "s_nop 0\n"
  "global_load_lds_dwordx4 v23, s[96:97]\n"
  "s_waitcnt vmcnt(24)\n"
  "s_barrier\n"
  "ds_read_b64_tr_b16 v[160:161], v120 offset:32768\n"
  "ds_read_b64_tr_b16 v[162:163], v120 offset:33024\n"
  "ds_read_b64_tr_b16 v[164:165], v120 offset:36864\n"
  "ds_read_b64_tr_b16 v[166:167], v120 offset:37120\n"
  "ds_read_b64_tr_b16 v[168:169], v120 offset:32800\n"
  "ds_read_b64_tr_b16 v[170:171], v120 offset:33056\n"
  "ds_read_b64_tr_b16 v[172:173], v120 offset:36896\n"
  "ds_read_b64_tr_b16 v[174:175], v120 offset:37152\n"
  "s_waitcnt lgkmcnt(0)\n"
  "ds_read_b64_tr_b16 v[176:177], v120 offset:49152\n"
  "ds_read_b64_tr_b16 v[178:179], v120 offset:49408\n"
  "ds_read_b64_tr_b16 v[180:181], v120 offset:53248\n"
  "ds_read_b64_tr_b16 v[182:183], v120 offset:53504\n"
  "ds_read_b64_tr_b16 v[184:185], v120 offset:49184\n"
  "ds_read_b64_tr_b16 v[186:187], v120 offset:49440\n"
  "ds_read_b64_tr_b16 v[188:189], v120 offset:53280\n"
  "ds_read_b64_tr_b16 v[190:191], v120 offset:53536\n"
  "s_waitcnt lgkmcnt(0)\n"
  "s_barrier\n"
  "s_lshl_b32 s58, s48, 13\n"
  "s_add_u32 m0, s58, 32768\n"
  "s_add_u32 s96, s46, 32768\n"
  "s_addc_u32 s97, s47, 0\n"
  "global_load_lds_dwordx4 v23, s[96:97]\n"
  "s_add_u32 s96, s96, 4096\n"
  "s_addc_u32 s97, s97, 0\n"
  "s_add_u32 m0, m0, 1024\n"
  "s_nop 0\n"
  "global_load_lds_dwordx4 v23, s[96:97]\n"
  "s_add_u32 s96, s96, 4096\n"
  "s_addc_u32 s97, s97, 0\n"
  "s_add_u32 m0, m0, 1024\n"
  "s_nop 0\n"
  "global_load_lds_dwordx4 v23, s[96:97]\n"
  "s_add_u32 s96, s96, 4096\n"
  "s_addc_u32 s97, s97, 0\n"
  "s_add_u32 m0, m0, 1024\n"
  "s_nop 0\n"
  "global_load_lds_dwordx4 v23, s[96:97]\n"
  "s_add_u32 s96, s96, 4096\n"
  "s_addc_u32 s97, s97, 0\n"
  "s_add_u32 m0, m0, 1024\n"
  "s_nop 0\n"
  "global_load_lds_dwordx4 v23, s[96:97]\n"
  "s_add_u32 s96, s96, 4096\n"
  "s_addc_u32 s97, s97, 0\n"
  "s_add_u32 m0, m0, 1024\n"
  "s_nop 0\n"
  "global_load_lds_dwordx4 v23, s[96:97]\n"
  "s_add_u32 s96, s96, 4096\n"
  "s_addc_u32 s97, s97, 0\n"
  "s_add_u32 m0, m0, 1024\n"
  "s_nop 0\n"
  "global_load_lds_dwordx4 v23, s[96:97]\n"
  "s_add_u32 s96, s96, 4096\n"
  "s_addc_u32 s97, s97, 0\n"
  "s_add_u32 m0, m0, 1024\n"
  "s_nop 0\n"
  "global_load_lds_dwordx4 v23, s[96:97]\n"
  "s_waitcnt vmcnt(24)\n"
  "s_barrier\n"
  "ds_read_b64_tr_b16 v[192:193], v123 offset:0\n"
  "ds_read_b64_tr_b16 v[194:195], v123 offset:256\n"
  "ds_read_b64_tr_b16 v[196:197], v123 offset:4096\n"
  "ds_read_b64_tr_b16 v[198:199], v123 offset:4352\n"
  "ds_read_b64_tr_b16 v[200:201], v123 offset:32\n"
  "ds_read_b64_tr_b16 v[202:203], v123 offset:288\n"
  "ds_read_b64_tr_b16 v[204:205], v123 offset:4128\n"
  "ds_read_b64_tr_b16 v[206:207], v123 offset:4384\n"
  "s_waitcnt lgkmcnt(0)\n"
  "ds_read_b64_tr_b16 v[208:209], v123 offset:16384\n"
  "ds_read_b64_tr_b16 v[210:211], v123 offset:16640\n"
  "ds_read_b64_tr_b16 v[212:213], v123 offset:20480\n"
  "ds_read_b64_tr_b16 v[214:215], v123 offset:20736\n"
  "ds_read_b64_tr_b16 v[216:217], v123 offset:16416\n"
  "ds_read_b64_tr_b16 v[218:219], v123 offset:16672\n"
  "ds_read_b64_tr_b16 v[220:221], v123 offset:20512\n"
  "ds_read_b64_tr_b16 v[222:223], v123 offset:20768\n"
  "s_waitcnt lgkmcnt(0)\n"
  "s_barrier\n"
  "s_lshl_b32 s58, s48, 13\n"
  "s_add_u32 m0, s58, 65536\n"
  "s_add_u32 s96, s46, 65536\n"
  "s_addc_u32 s97, s47, 0\n"
  "global_load_lds_dwordx4 v23, s[96:97]\n"
  "s_add_u32 s96, s96, 4096\n"
  "s_addc_u32 s97, s97, 0\n"
  "s_add_u32 m0, m0, 1024\n"
  "s_nop 0\n"
  "global_load_lds_dwordx4 v23, s[96:97]\n"
  "s_add_u32 s96, s96, 4096\n"
  "s_addc_u32 s97, s97, 0\n"
  "s_add_u32 m0, m0, 1024\n"
  "s_nop 0\n"
  "global_load_lds_dwordx4 v23, s[96:97]\n"
  "s_add_u32 s96, s96, 4096\n"
  "s_addc_u32 s97, s97, 0\n"
  "s_add_u32 m0, m0, 1024\n"
  "s_nop 0\n"
  "global_load_lds_dwordx4 v23, s[96:97]\n"
  "s_add_u32 s96, s96, 4096\n"
  "s_addc_u32 s97, s97, 0\n"
  "s_add_u32 m0, m0, 1024\n"
  "s_nop 0\n"
  "global_load_lds_dwordx4 v23, s[96:97]\n"
  "s_add_u32 s96, s96, 4096\n"
  "s_addc_u32 s97, s97, 0\n"
  "s_add_u32 m0, m0, 1024\n"
  "s_nop 0\n"
  "global_load_lds_dwordx4 v23, s[96:97]\n"
  "s_add_u32 s96, s96, 4096\n"
  "s_addc_u32 s97, s97, 0\n"
  "s_add_u32 m0, m0, 1024\n"
  "s_nop 0\n"
  "global_load_lds_dwordx4 v23, s[96:97]\n"
  "s_add_u32 s96, s96, 4096\n"
  "s_addc_u32 s97, s97, 0\n"
  "s_add_u32 m0, m0, 1024\n"
  "s_nop 0\n"
  "global_load_lds_dwordx4 v23, s[96:97]\n"
  "s_waitcnt vmcnt(24)\n"
  "s_barrier\n"
  "ds_read_b64_tr_b16 v[224:225], v123 offset:32768\n"
  "ds_read_b64_tr_b16 v[226:227], v123 offset:33024\n"
  "ds_read_b64_tr_b16 v[228:229], v123 offset:36864\n"
  "ds_read_b64_tr_b16 v[230:231], v123 offset:37120\n"
  "ds_read_b64_tr_b16 v[232:233], v123 offset:32800\n"
  "ds_read_b64_tr_b16 v[234:235], v123 offset:33056\n"
  "ds_read_b64_tr_b16 v[236:237], v123 offset:36896\n"
  "ds_read_b64_tr_b16 v[238:239], v123 offset:37152\n"
  "s_waitcnt lgkmcnt(0)\n"
  "ds_read_b64_tr_b16 v[240:241], v123 offset:49152\n"
  "ds_read_b64_tr_b16 v[242:243], v123 offset:49408\n"
  "ds_read_b64_tr_b16 v[244:245], v123 offset:53248\n"
  "ds_read_b64_tr_b16 v[246:247], v123 offset:53504\n"
  "ds_read_b64_tr_b16 v[248:249], v123 offset:49184\n"
  "ds_read_b64_tr_b16 v[250:251], v123 offset:49440\n"
  "ds_read_b64_tr_b16 v[252:253], v123 offset:53280\n"
  "ds_read_b64_tr_b16 v[254:255], v123 offset:53536\n"
  "s_waitcnt lgkmcnt(0)\n"
  "s_barrier\n"
  "s_lshl_b32 s58, s48, 13\n"
  "s_add_u32 m0, s58, 98304\n"
  "s_add_u32 s96, s46, 98304\n"
  "s_addc_u32 s97, s47, 0\n"
  "global_load_lds_dwordx4 v23, s[96:97]\n"
  "s_add_u32 s96, s96, 4096\n"
  "s_addc_u32 s97, s97, 0\n"
  "s_add_u32 m0, m0, 1024\n"
  "s_nop 0\n"
  "global_load_lds_dwordx4 v23, s[96:97]\n"
  "s_add_u32 s96, s96, 4096\n"
  "s_addc_u32 s97, s97, 0\n"
  "s_add_u32 m0, m0, 1024\n"
  "s_nop 0\n"
  "global_load_lds_dwordx4 v23, s[96:97]\n"
  "s_add_u32 s96, s96, 4096\n"
  "s_addc_u32 s97, s97, 0\n"
  "s_add_u32 m0, m0, 1024\n"
  "s_nop 0\n"
  "global_load_lds_dwordx4 v23, s[96:97]\n"
  "s_add_u32 s96, s96, 4096\n"
  "s_addc_u32 s97, s97, 0\n"
  "s_add_u32 m0, m0, 1024\n"
  "s_nop 0\n"
  "global_load_lds_dwordx4 v23, s[96:97]\n"
  "s_add_u32 s96, s96, 4096\n"
  "s_addc_u32 s97, s97, 0\n"
  "s_add_u32 m0, m0, 1024\n"
  "s_nop 0\n"
  "global_load_lds_dwordx4 v23, s[96:97]\n"
  "s_add_u32 s96, s96, 4096\n"
  "s_addc_u32 s97, s97, 0\n"
  "s_add_u32 m0, m0, 1024\n"
  "s_nop 0\n"
  "global_load_lds_dwordx4 v23, s[96:97]\n"
  "s_add_u32 s96, s96, 4096\n"
  "s_addc_u32 s97, s97, 0\n"
  "s_add_u32 m0, m0, 1024\n"
  "s_nop 0\n"
  "global_load_lds_dwordx4 v23, s[96:97]\n"
  "s_waitcnt vmcnt(16)\n"
  "ds_read_b64_tr_b16 a[0:1], v20 offset:0\n"
  "ds_read_b64_tr_b16 a[2:3], v20 offset:512\n"
  "ds_read_b64_tr_b16 a[4:5], v20 offset:32768\n"
  "ds_read_b64_tr_b16 a[6:7], v20 offset:33280\n"
  "ds_read_b64_tr_b16 a[16:17], v21 offset:0\n"
  "ds_read_b64_tr_b16 a[18:19], v21 offset:512\n"
  "ds_read_b64_tr_b16 a[20:21], v21 offset:32768\n"
  "ds_read_b64_tr_b16 a[22:23], v21 offset:33280\n"
  "s_waitcnt lgkmcnt(0)\n"
  "ds_read_b64_tr_b16 a[128:129], v20 offset:64\n"
  "ds_read_b64_tr_b16 a[130:131], v20 offset:576\n"
  "ds_read_b64_tr_b16 a[132:133], v20 offset:32832\n"
  "ds_read_b64_tr_b16 a[134:135], v20 offset:33344\n"
  "ds_read_b64_tr_b16 a[144:145], v21 offset:64\n"
  "ds_read_b64_tr_b16 a[146:147], v21 offset:576\n"
  "ds_read_b64_tr_b16 a[148:149], v21 offset:32832\n"
  "ds_read_b64_tr_b16 a[150:151], v21 offset:33344\n"
  "s_waitcnt lgkmcnt(0)\n"
  "ds_read_b64_tr_b16 v[88:89], v20 offset:128\n"
  "ds_read_b64_tr_b16 v[90:91], v20 offset:640\n"
  "ds_read_b64_tr_b16 v[92:93], v20 offset:32896\n"
  "ds_read_b64_tr_b16 v[94:95], v20 offset:33408\n"
  "ds_read_b64_tr_b16 v[96:97], v21 offset:128\n"
  "ds_read_b64_tr_b16 v[98:99], v21 offset:640\n"
  "ds_read_b64_tr_b16 v[100:101], v21 offset:32896\n"
  "ds_read_b64_tr_b16 v[102:103], v21 offset:33408\n"
  "s_waitcnt lgkmcnt(0)\n"
  "ds_read_b64_tr_b16 v[104:105], v20 offset:192\n"
  "ds_read_b64_tr_b16 v[106:107], v20 offset:704\n"
  "ds_read_b64_tr_b16 v[108:109], v20 offset:32960\n"
  "ds_read_b64_tr_b16 v[110:111], v20 offset:33472\n"
  "ds_read_b64_tr_b16 v[112:113], v21 offset:192\n"
  "ds_read_b64_tr_b16 v[114:115], v21 offset:704\n"
  "ds_read_b64_tr_b16 v[116:117], v21 offset:32960\n"
  "ds_read_b64_tr_b16 v[118:119], v21 offset:33472\n"
  "s_waitcnt lgkmcnt(0)\n"
  "ds_write_b128 v17, v[24:27] offset:0\n"
  "ds_write_b128 v17, v[28:31] offset:1024\n"
  "ds_write_b128 v17, v[32:35] offset:2048\n"
  "ds_write_b128 v17, v[36:39] offset:3072\n"
  "ds_write_b128 v17, v[40:43] offset:4096\n"
  "ds_write_b128 v17, v[44:47] offset:5120\n"
  "ds_write_b128 v17, v[48:51] offset:6144\n"
  "ds_write_b128 v17, v[52:55] offset:7168\n"
  "ds_write_b128 v17, v[88:91] offset:32768\n"
  "ds_write_b128 v17, v[92:95] offset:33792\n"
  "ds_write_b128 v17, v[96:99] offset:34816\n"
  "ds_write_b128 v17, v[100:103] offset:35840\n"
  "ds_write_b128 v17, v[104:107] offset:36864\n"
  "ds_write_b128 v17, v[108:111] offset:37888\n"
  "ds_write_b128 v17, v[112:115] offset:38912\n"
  "ds_write_b128 v17, v[116:119] offset:39936\n"
  "s_waitcnt lgkmcnt(0)\n"
  "s_waitcnt vmcnt(0)\n"
  "ds_read_b64_tr_b16 a[32:33], v124 offset:0\n"
  "ds_read_b64_tr_b16 a[34:35], v124 offset:512\n"
  "ds_read_b64_tr_b16 a[36:37], v124 offset:32768\n"
  "ds_read_b64_tr_b16 a[38:39], v124 offset:33280\n"
  "ds_read_b64_tr_b16 a[48:49], v125 offset:0\n"
  "ds_read_b64_tr_b16 a[50:51], v125 offset:512\n"
  "ds_read_b64_tr_b16 a[52:53], v125 offset:32768\n"
  "ds_read_b64_tr_b16 a[54:55], v125 offset:33280\n"
  "s_waitcnt lgkmcnt(0)\n"
  "ds_read_b64_tr_b16 a[160:161], v124 offset:64\n"
  "ds_read_b64_tr_b16 a[162:163], v124 offset:576\n"
  "ds_read_b64_tr_b16 a[164:165], v124 offset:32832\n"
  "ds_read_b64_tr_b16 a[166:167], v124 offset:33344\n"
  "ds_read_b64_tr_b16 a[176:177], v125 offset:64\n"
  "ds_read_b64_tr_b16 a[178:179], v125 offset:576\n"
  "ds_read_b64_tr_b16 a[180:181], v125 offset:32832\n"
  "ds_read_b64_tr_b16 a[182:183], v125 offset:33344\n"
  "s_waitcnt lgkmcnt(0)\n"
  "ds_read_b64_tr_b16 v[88:89], v124 offset:128\n"
  "ds_read_b64_tr_b16 v[90:91], v124 offset:640\n"
  "ds_read_b64_tr_b16 v[92:93], v124 offset:32896\n"
  "ds_read_b64_tr_b16 v[94:95], v124 offset:33408\n"
  "ds_read_b64_tr_b16 v[96:97], v125 offset:128\n"
  "ds_read_b64_tr_b16 v[98:99], v125 offset:640\n"
  "ds_read_b64_tr_b16 v[100:101], v125 offset:32896\n"
  "ds_read_b64_tr_b16 v[102:103], v125 offset:33408\n"
  "s_waitcnt lgkmcnt(0)\n"
  "ds_read_b64_tr_b16 v[104:105], v124 offset:192\n"
  "ds_read_b64_tr_b16 v[106:107], v124 offset:704\n"
  "ds_read_b64_tr_b16 v[108:109], v124 offset:32960\n"
  "ds_read_b64_tr_b16 v[110:111], v124 offset:33472\n"
  "ds_read_b64_tr_b16 v[112:113], v125 offset:192\n"
  "ds_read_b64_tr_b16 v[114:115], v125 offset:704\n"
  "ds_read_b64_tr_b16 v[116:117], v125 offset:32960\n"
  "ds_read_b64_tr_b16 v[118:119], v125 offset:33472\n"
  "s_waitcnt lgkmcnt(0)\n"
  "ds_write_b128 v5, v[56:59] offset:0\n"
  "ds_write_b128 v5, v[60:63] offset:1024\n"
  "ds_write_b128 v5, v[64:67] offset:2048\n"
  "ds_write_b128 v5, v[68:71] offset:3072\n"
  "ds_write_b128 v5, v[72:75] offset:4096\n"
  "ds_write_b128 v5, v[76:79] offset:5120\n"
  "ds_write_b128 v5, v[80:83] offset:6144\n"
  "ds_write_b128 v5, v[84:87] offset:7168\n"
  "ds_write_b128 v5, v[88:91] offset:32768\n"
  "ds_write_b128 v5, v[92:95] offset:33792\n"
  "ds_write_b128 v5, v[96:99] offset:34816\n"
  "ds_write_b128 v5, v[100:103] offset:35840\n"
  "ds_write_b128 v5, v[104:107] offset:36864\n"
  "ds_write_b128 v5, v[108:111] offset:37888\n"
  "ds_write_b128 v5, v[112:115] offset:38912\n"
  "ds_write_b128 v5, v[116:119] offset:39936\n"
  "s_waitcnt lgkmcnt(0)\n"
  "s_mov_b32 s59, 0\n"
  "v_mov_b32 v24, s49\n"
  "POLW%=:\n"
  "buffer_load_dword v25, v24, s[36:39], 0 offen sc1\n"
  "s_add_u32 s59, s59, 1\n"
  "s_cmp_lt_u32 s59, 0x20000\n"
  "s_cbranch_scc0 END%=\n"
  "s_waitcnt vmcnt(0)\n"
  "v_readfirstlane_b32 s96, v25\n"
  "s_cmp_eq_u32 s96, 0\n"
  "s_cbranch_scc1 POLW%=\n"
  "s_sub_u32 s96, s96, 1\n"
  "s_and_b32 s96, s96, 1\n"
  "s_lshl_b32 s49, s96, 13\n"
  "s_mov_b32 exec_lo, 0\n"
  "s_mov_b32 exec_hi, 3\n"
  "v_subrev_u32 v3, s49, v3\n"
  "s_mov_b64 exec, -1\n"
  "v_mov_b32 v18, 0x3F803F80\n"
  "v_mov_b32 v19, 0x3F803F80\n"
  "v_mov_b32 v20, 0x3F803F80\n"
  "v_mov_b32 v21, 0x3F803F80\n"
  "v_mov_b32 v22, 0x3F003F00\n"
  "v_mov_b32 v23, 0x3F003F00\n"
  "s_mov_b32 s53, 0\n"
  "s_mov_b32 s47, %8\n"
  "s_memtime s[96:97]\n"
  "s_waitcnt lgkmcnt(0)\n"
  "s_mov_b32 s46, s96\n"
  "s_add_u32 s98, s49, 1048576\n"
  "s_xor_b32 s54, s98, 8192\n"
  "s_mov_b32 s55, s49\n"
  "s_lshl_b32 s56, s47, 20\n"
  "s_add_u32 s57, s56, 38805504\n"
  "s_add_u32 s56, s56, 5242880\n"
  "s_lshl_b32 s61, s47, 14\n"
  "s_add_u32 s61, s61, s49\n"
  "s_lshl_b32 s65, s48, 11\n"
  "s_add_u32 s65, s65, 131072\n"
  "v_mov_b32 v112, 0\n"
  "v_mov_b32 v113, 0\n"
  "v_mov_b32 v114, 0\n"
  "v_mov_b32 v115, 0\n"
  "s_barrier\n"
  "s_mov_b32 s59, 0\n"
  "KV0%=:\n"
  "s_sleep 2\n"
  "buffer_load_dword v24, v3, s[36:39], s98 offen sc1\n"
  "s_add_u32 s59, s59, 1\n"
  "s_cmp_lt_u32 s59, 0x20000\n"
  "s_cbranch_scc0 END%=\n"
  "s_waitcnt vmcnt(0)\n"
  "v_readlane_b32 s96, v24, 32\n"
  "v_readlane_b32 s97, v24, 33\n"
  "s_add_u32 s42, s53, 1\n"
  "s_min_u32 s42, s42, 63\n"
  "s_lshr_b32 s42, s42, 1\n"
  "s_and_b32 s96, s96, s97\n"
  "s_bitcmp1_b32 s96, s42\n"
  "s_cbranch_scc0 KV0%=\n"
  "s_add_u32 m0, s65, 8192\n"
  "s_nop 0\n"
  "buffer_load_dwordx4 v1, s[36:39], s56 offen offset:0 sc1 lds\n"
  "buffer_load_dwordx4 v1, s[36:39], s56 offen offset:1024 sc1 lds\n"
  "s_memrealtime s[96:97]\n"
  "s_memtime s[44:45]\n"
  "ds_read_b128 v[40:43], v17 offset:0\n"
  "ds_read_b128 v[44:47], v17 offset:1024\n"
  "ds_read_b128 v[48:51], v17 offset:2048\n"
  "ds_read_b128 v[52:55], v17 offset:3072\n"
  "ds_read_b128 v[56:59], v17 offset:4096\n"
  "ds_read_b128 v[60:63], v17 offset:5120\n"
  "ds_read_b128 v[64:67], v17 offset:6144\n"
  "ds_read_b128 v[68:71], v17 offset:7168\n"
  "ds_read_b128 v[72:75], v5 offset:0\n"
  "ds_read_b128 v[76:79], v5 offset:1024\n"
  "ds_read_b128 v[80:83], v5 offset:2048\n"
  "ds_read_b128 v[84:87], v5 offset:3072\n"
  "ds_read_b128 v[88:91], v5 offset:4096\n"
  "ds_read_b128 v[92:95], v5 offset:5120\n"
  "ds_read_b128 v[96:99], v5 offset:6144\n"
  "ds_read_b128 v[100:103], v5 offset:7168\n"
  "v_mov_b32 v104, 0\n"
  "v_mov_b32 v105, 0\n"
  "v_mov_b32 v106, 0\n"
  "v_mov_b32 v107, 0\n"
  "v_mov_b32 v108, 0\n"
  "v_mov_b32 v109, 0\n"
  "v_mov_b32 v110, 0\n"
  "v_mov_b32 v111, 0\n"
  "v_mov_b32 v116, 0\n"
  "v_mov_b32 v117, 0\n"
  "v_mov_b32 v118, 0\n"
  "v_mov_b32 v119, 0\n"
  "s_waitcnt lgkmcnt(0)\n"
  "s_mul_i32 s93, s96, 23\n"
  "s_sub_u32 s93, s93, s44\n"
  "s_nop 0\n"
  "LOOP%=:\n"
  "v_mov_b32 v6, s52\n"
  "v_mov_b32 v7, s52\n"
  "v_mov_b32 v8, s52\n"
  "v_mov_b32 v9, s52\n"
  "s_mov_b32 s59, 0\n"
  "s_xor_b32 s43, s61, 524288\n"
  "s_add_u32 s46, s46, s51\n"
  "s_mov_b32 m0, s65\n"
  "D2_c%=:\n"
  "s_memtime s[96:97]\n"
  "s_getpc_b64 s[40:41]\n"
  "D2_pc%=:\n"
  "s_waitcnt lgkmcnt(0)\n"
  "s_sub_i32 s96, s46, s96\n"
  "s_cmp_gt_i32 s96, 448\n"
  "s_cbranch_scc1 D2_c%=\n"
  "s_max_i32 s96, s96, 0\n"
  "s_and_b32 s96, s96, 0x1fc\n"
  "s_sub_u32 s96, D2_e%=-D2_pc%=, s96\n"
  "s_add_u32 s40, s40, s96\n"
  "s_addc_u32 s41, s41, 0\n"
  "s_setpc_b64 s[40:41]\n"
  "s_nop 0\n"
  "s_nop 0\n"
  "s_nop 0\n"
  "s_nop 0\n"
  "s_nop 0\n"
  "s_nop 0\n"
  "s_nop 0\n"
  "s_nop 0\n"
  "s_nop 0\n"
  "s_nop 0\n"
  "s_nop 0\n"
  "s_nop 0\n"
  "s_nop 0\n"
  "s_nop 0\n"
  "s_nop 0\n"
  "s_nop 0\n"
  "s_nop 0\n"
  "s_nop 0\n"
  "s_nop 0\n"
  "s_nop 0\n"
  "s_nop 0\n"
  "s_nop 0\n"
  "s_nop 0\n"
  "s_nop 0\n"
  "s_nop 0\n"
  "s_nop 0\n"
  "s_nop 0\n"
  "s_nop 0\n"
  "s_nop 0\n"
  "s_nop 0\n"
  "s_nop 0\n"
  "s_nop 0\n"
  "s_nop 0\n"
  "s_nop 0\n"
  "s_nop 0\n"
  "s_nop 0\n"
  "s_nop 0\n"
  "s_nop 0\n"
  "s_nop 0\n"
  "s_nop 0\n"
  "s_nop 0\n"
  "s_nop 0\n"
  "s_nop 0\n"
  "s_nop 0\n"
  "s_nop 0\n"
  "s_nop 0\n"
  "s_nop 0\n"
  "s_nop 0\n"
  "s_nop 0\n"
  "s_nop 0\n"
  "s_nop 0\n"
  "s_nop 0\n"
  "s_nop 0\n"
  "s_nop 0\n"
  "s_nop 0\n"
  "s_nop 0\n"
  "s_nop 0\n"
  "s_nop 0\n"
  "s_nop 0\n"
  "s_nop 0\n"
  "s_nop 0\n"
  "s_nop 0\n"
  "s_nop 0\n"
  "s_nop 0\n"
  "s_nop 0\n"
  "s_nop 0\n"
  "s_nop 0\n"
  "s_nop 0\n"
  "s_nop 0\n"
  "s_nop 0\n"
  "s_nop 0\n"
  "s_nop 0\n"
  "s_nop 0\n"
  "s_nop 0\n"
  "s_nop 0\n"
  "s_nop 0\n"
  "s_nop 0\n"
  "s_nop 0\n"
  "s_nop 0\n"
  "s_nop 0\n"
  "s_nop 0\n"
  "s_nop 0\n"
  "s_nop 0\n"
  "s_nop 0\n"
  "s_nop 0\n"
  "s_nop 0\n"
  "s_nop 0\n"
  "s_nop 0\n"
  "s_nop 0\n"
  "s_nop 0\n"
  "s_nop 0\n"
  "s_nop 0\n"
  "s_nop 0\n"
  "s_nop 0\n"
  "s_nop 0\n"
  "s_nop 0\n"
  "s_nop 0\n"
  "s_nop 0\n"
  "s_nop 0\n"
  "s_nop 0\n"
  "s_nop 0\n"
  "s_nop 0\n"
  "s_nop 0\n"
  "s_nop 0\n"
  "s_nop 0\n"
  "s_nop 0\n"
  "s_nop 0\n"
  "s_nop 0\n"
  "s_nop 0\n"
  "s_nop 0\n"
  "s_nop 0\n"
  "s_nop 0\n"
  "s_nop 0\n"
  "D2_e%=:\n"
  "buffer_load_dwordx4 v1, s[36:39], s54 offen sc1 lds\n"
  "buffer_load_dwordx4 v1, s[36:39], s54 offen offset:1024 sc1 lds\n"
  "ds_read_b128 v[80:83], v5 offset:2048\n"
  "ds_read_b128 v[84:87], v5 offset:3072\n"
  "ds_read_b128 v[88:91], v5 offset:4096\n"
  "ds_read_b128 v[92:95], v5 offset:5120\n"
  "ds_read_b128 v[96:99], v5 offset:6144\n"
  "ds_read_b128 v[100:103], v5 offset:7168\n"
  "s_add_u32 s42, s56, 8192\n"
  "s_add_u32 s40, s56, 16384\n"
  "s_sub_u32 s41, s57, 16384\n"
  "s_cmp_eq_u32 s53, 0\n"
  "s_cselect_b32 s41, 0x7FFF0000, s41\n"
  "s_and_b32 s62, s53, 3\n"
  "s_cmp_eq_u32 s62, s48\n"
  "s_cselect_b32 s58, s98, 0x7FFF0000\n"
  "s_lshl_b32 s62, s62, 6\n"
  "s_add_u32 s62, s62, s98\n"
  "s_lshl_b32 s84, s53, 24\n"
  "s_waitcnt vmcnt(0) lgkmcnt(0)\n"
  "T0%=:\n"
  "s_barrier\n"
  "s_memtime s[44:45]\n"
  "ds_read_b128 v[24:27], v12 offset:0\n"
  "ds_read2st64_b32 v[124:125], v11 offset1:16\n"
  "ds_read_b128 v[28:31], v12 offset:1024\n"
  "ds_read_b128 v[32:35], v12 offset:2048\n"
  "s_add_u32 m0, s65, 8192\n"
  "s_waitcnt lgkmcnt(3)\n"
  "v_smfmac_f32_16x16x64_bf16 v[104:107], v[24:27], v[40:47], v0\n"
  "ds_read_b128 v[36:39], v12 offset:3072\n"
  "v_smfmac_f32_16x16x64_bf16 v[108:111], v[24:27], v[48:55], v0\n"
  "buffer_load_dwordx4 v[120:123], v1, s[36:39], s42 offen offset:0 sc1\n"
  "s_waitcnt lgkmcnt(2)\n"
  "v_smfmac_f32_16x16x64_bf16 v[104:107], v[28:31], v[56:63], v0\n"
  "v_max_u32 v124, v124, v125\n"
  "v_smfmac_f32_16x16x64_bf16 v[108:111], v[28:31], v[64:71], v0\n"
  "v_cmp_eq_u32 vcc, s52, v124\n"
  "s_waitcnt lgkmcnt(1)\n"
  "v_smfmac_f32_16x16x64_bf16 v[104:107], v[32:35], v[72:79], v0\n"
  "s_cbranch_vccnz CANF%=\n"
  "ds_read_b128 v[40:43], v12 offset:4096\n"
  "v_smfmac_f32_16x16x64_bf16 v[108:111], v[32:35], v[80:87], v0\n"
  "ds_read_b128 v[44:47], v12 offset:5120\n"
  "s_waitcnt lgkmcnt(2)\n"
  "v_smfmac_f32_16x16x64_bf16 v[104:107], v[36:39], v[88:95], v0\n"
  "ds_read_b128 v[48:51], v12 offset:6144\n"
  "v_smfmac_f32_16x16x64_bf16 v[108:111], v[36:39], v[96:103], v0\n"
  "s_add_u32 s96, s44, s93\n"
  "s_and_b32 s96, s96, 0xFFFFFF\n"
  "s_or_b32 s94, s96, s84\n"
  "v_mov_b32 v124, s94\n"
  "s_waitcnt lgkmcnt(2)\n"
  "v_smfmac_f32_16x16x64_bf16 v[104:107], v[40:43], a[64:71], v0\n"
  "ds_read_b128 v[52:55], v12 offset:7168\n"
  "v_smfmac_f32_16x16x64_bf16 v[108:111], v[40:43], a[192:199], v0\n"
  "buffer_store_dword v124, v4, s[36:39], s58 offen sc1\n"
  "s_waitcnt lgkmcnt(2)\n"
  "v_smfmac_f32_16x16x64_bf16 v[104:107], v[44:47], a[80:87], v0\n"
  "ds_read_b128 v[56:59], v10 offset:0\n"
  "v_smfmac_f32_16x16x64_bf16 v[108:111], v[44:47], a[208:215], v0\n"
  "ds_read_b128 v[72:75], v12 offset:8192\n"
  "s_waitcnt lgkmcnt(3)\n"
  "v_smfmac_f32_16x16x64_bf16 v[104:107], v[48:51], a[96:103], v0\n"
  "ds_read_b128 v[60:63], v10 offset:1024\n"
  "v_smfmac_f32_16x16x64_bf16 v[108:111], v[48:51], a[224:231], v0\n"
  "buffer_load_dwordx4 v[124:127], v1, s[36:39], s42 offen offset:1024 sc1\n"
  "s_waitcnt lgkmcnt(3)\n"
  "v_smfmac_f32_16x16x64_bf16 v[104:107], v[52:55], a[112:119], v0\n"
  "ds_read_b128 v[64:67], v10 offset:2048\n"
  "v_smfmac_f32_16x16x64_bf16 v[108:111], v[52:55], a[240:247], v0\n"
  "ds_read_b128 v[76:79], v12 offset:9216\n"
  "s_waitcnt lgkmcnt(4)\n"
  "v_smfmac_f32_16x16x64_bf16 v[104:107], v[56:59], a[8:15], v0\n"
  "ds_read_b128 v[68:71], v10 offset:3072\n"
  "v_smfmac_f32_16x16x64_bf16 v[108:111], v[56:59], a[136:143], v0\n"
  "buffer_store_dwordx4 v[6:9], v1, s[36:39], s43 offen offset:0 sc1\n"
  "s_waitcnt lgkmcnt(3)\n"
  "v_smfmac_f32_16x16x64_bf16 v[104:107], v[60:63], a[24:31], v0\n"
  "ds_read_b128 v[56:59], v10 offset:4096\n"
  "v_smfmac_f32_16x16x64_bf16 v[108:111], v[60:63], a[152:159], v0\n"
  "ds_read_b128 v[80:83], v12 offset:10240\n"
  "s_waitcnt lgkmcnt(4)\n"
  "v_smfmac_f32_16x16x64_bf16 v[104:107], v[64:67], a[40:47], v0\n"
  "ds_read_b128 v[60:63], v10 offset:5120\n"
  "v_smfmac_f32_16x16x64_bf16 v[108:111], v[64:67], a[168:175], v0\n"
  "buffer_store_dwordx4 v[6:9], v1, s[36:39], s43 offen offset:1024 sc1\n"
  "s_waitcnt lgkmcnt(3)\n"
  "v_smfmac_f32_16x16x64_bf16 v[104:107], v[68:71], a[56:63], v0\n"
  "ds_read_b128 v[64:67], v10 offset:6144\n"
  "v_smfmac_f32_16x16x64_bf16 v[108:111], v[68:71], a[184:191], v0\n"
  "ds_read_b128 v[84:87], v12 offset:11264\n"
  "s_waitcnt lgkmcnt(4)\n"
  "v_smfmac_f32_16x16x64_bf16 v[104:107], v[56:59], a[72:79], v0\n"
  "ds_read_b128 v[68:71], v10 offset:7168\n"
  "v_smfmac_f32_16x16x64_bf16 v[108:111], v[56:59], a[200:207], v0\n"
  "buffer_store_dwordx4 v[112:115], v1, s[36:39], s41 offen offset:1024 sc1\n"
  "s_waitcnt lgkmcnt(3)\n"
  "v_smfmac_f32_16x16x64_bf16 v[104:107], v[60:63], a[88:95], v0\n"
  "ds_read_b128 v[88:91], v12 offset:12288\n"
  "v_smfmac_f32_16x16x64_bf16 v[108:111], v[60:63], a[216:223], v0\n"
  "ds_read_b128 v[92:95], v12 offset:13312\n"
  "s_waitcnt lgkmcnt(4)\n"
  "v_smfmac_f32_16x16x64_bf16 v[104:107], v[64:67], a[104:111], v0\n"
  "v_mov_b32 v112, 0\n"
  "v_smfmac_f32_16x16x64_bf16 v[108:111], v[64:67], a[232:239], v0\n"
  "v_mov_b32 v113, 0\n"
  "s_waitcnt lgkmcnt(2)\n"
  "v_smfmac_f32_16x16x64_bf16 v[104:107], v[68:71], a[120:127], v0\n"
  "v_mov_b32 v114, 0\n"
  "v_smfmac_f32_16x16x64_bf16 v[108:111], v[68:71], a[248:255], v0\n"
  "v_mov_b32 v115, 0\n"
  "v_smfmac_f32_16x16x64_bf16 v[116:119], v[24:27], v[136:143], v0\n"
  "s_nop 3\n"
  "v_cvt_pk_bf16_f32 v8, v104, v105\n"
  "v_smfmac_f32_16x16x64_bf16 v[112:115], v[24:27], v[128:135], v0\n"
  "v_cvt_pk_bf16_f32 v9, v106, v107\n"
  "ds_write_b64 v14, v[8:9]\n"
  "v_cvt_pk_bf16_f32 v6, v108, v109\n"
  "v_cvt_pk_bf16_f32 v7, v110, v111\n"
  "v_smfmac_f32_16x16x64_bf16 v[116:119], v[28:31], v[152:159], v0\n"
  "ds_write_b64 v14, v[6:7] offset:2304\n"
  "v_smfmac_f32_16x16x64_bf16 v[112:115], v[28:31], v[144:151], v0\n"
  "ds_read_b128 v[96:99], v12 offset:14336\n"
  "v_smfmac_f32_16x16x64_bf16 v[116:119], v[32:35], v[168:175], v0\n"
  "ds_read_b128 v[100:103], v12 offset:15360\n"
  "v_smfmac_f32_16x16x64_bf16 v[112:115], v[32:35], v[160:167], v0\n"
  "v_smfmac_f32_16x16x64_bf16 v[116:119], v[36:39], v[184:191], v0\n"
  "s_waitcnt lgkmcnt(0)\n"
  "s_barrier\n"
  "ds_read_b64_tr_b16 v[56:57], v15 offset:0\n"
  "ds_read_b64_tr_b16 v[58:59], v15 offset:128\n"
  "v_smfmac_f32_16x16x64_bf16 v[112:115], v[36:39], v[176:183], v0\n"
  "ds_read_b64_tr_b16 v[60:61], v15 offset:256\n"
  "ds_read_b64_tr_b16 v[62:63], v15 offset:384\n"
  "v_smfmac_f32_16x16x64_bf16 v[116:119], v[40:43], v[200:207], v0\n"
  "ds_read_b64_tr_b16 v[64:65], v15 offset:512\n"
  "ds_read_b64_tr_b16 v[66:67], v15 offset:640\n"
  "v_smfmac_f32_16x16x64_bf16 v[112:115], v[40:43], v[192:199], v0\n"
  "ds_read_b64_tr_b16 v[68:69], v15 offset:768\n"
  "ds_read_b64_tr_b16 v[70:71], v15 offset:896\n"
  "v_smfmac_f32_16x16x64_bf16 v[116:119], v[44:47], v[216:223], v0\n"
  "s_waitcnt lgkmcnt(6)\n"
  "v_mfma_f32_16x16x32_bf16 v[104:107], v[72:75], v[56:59], 0\n"
  "ds_read_b64_tr_b16 v[24:25], v15 offset:2304\n"
  "ds_read_b64_tr_b16 v[26:27], v15 offset:2432\n"
  "s_waitcnt lgkmcnt(6)\n"
  "v_mfma_f32_16x16x32_bf16 v[104:107], v[76:79], v[60:63], v[104:107]\n"
  "ds_read_b64_tr_b16 v[28:29], v15 offset:2560\n"
  "ds_read_b64_tr_b16 v[30:31], v15 offset:2688\n"
  "s_waitcnt lgkmcnt(6)\n"
  "v_mfma_f32_16x16x32_bf16 v[104:107], v[80:83], v[64:67], v[104:107]\n"
  "ds_read_b64_tr_b16 v[32:33], v15 offset:2816\n"
  "ds_read_b64_tr_b16 v[34:35], v15 offset:2944\n"
  "s_waitcnt lgkmcnt(6)\n"
  "v_mfma_f32_16x16x32_bf16 v[104:107], v[84:87], v[68:71], v[104:107]\n"
  "ds_read_b64_tr_b16 v[36:37], v15 offset:3072\n"
  "ds_read_b64_tr_b16 v[38:39], v15 offset:3200\n"
  "v_mfma_f32_16x16x32_bf16 v[108:111], v[88:91], v[56:59], 0\n"
  "ds_read_b128 v[80:83], v17 offset:32768\n"
  "v_mfma_f32_16x16x32_bf16 v[108:111], v[92:95], v[60:63], v[108:111]\n"
  "ds_read_b128 v[84:87], v17 offset:33792\n"
  "v_mfma_f32_16x16x32_bf16 v[108:111], v[96:99], v[64:67], v[108:111]\n"
  "s_nop 0\n"
  "v_cmp_u_f32 vcc, v104, v104\n"
  "v_mfma_f32_16x16x32_bf16 v[108:111], v[100:103], v[68:71], v[108:111]\n"
  "s_cbranch_vccnz SLOWB%=\n"
  "CONTB%=:\n"
  "s_waitcnt lgkmcnt(8)\n"
  "v_mfma_f32_16x16x32_bf16 v[72:75], v[24:27], v[56:59], 0\n"
  "v_max3_f32 v8, v104, v105, v106\n"
  "s_waitcnt lgkmcnt(6)\n"
  "v_mfma_f32_16x16x32_bf16 v[72:75], v[28:31], v[60:63], v[72:75]\n"
  "buffer_store_dwordx2 v[6:7], v13, s[36:39], s57 offen sc1\n"
  "s_waitcnt lgkmcnt(4)\n"
  "v_mfma_f32_16x16x32_bf16 v[72:75], v[32:35], v[64:67], v[72:75]\n"
  "v_max3_f32 v8, v8, v107, v108\n"
  "v_max3_f32 v9, v109, v110, v111\n"
  "s_waitcnt lgkmcnt(2)\n"
  "v_mfma_f32_16x16x32_bf16 v[72:75], v[36:39], v[68:71], v[72:75]\n"
  "v_max_f32 v8, v8, v9\n"
  "v_max_f32 v9, v8, v9\n"
  "v_smfmac_f32_16x16x64_bf16 v[112:115], v[44:47], v[208:215], v0\n"
  "ds_read_b128 v[88:91], v17 offset:36864\n"
  "v_permlane32_swap_b32 v8, v9\n"
  "v_smfmac_f32_16x16x64_bf16 v[116:119], v[48:51], v[232:239], v0\n"
  "buffer_load_dwordx4 v1, s[36:39], s40 offen offset:0 sc1 lds\n"
  "s_nop 0\n"
  "v_max3_f32 v8, v8, v9, v72\n"
  "v_max3_f32 v9, v73, v74, v75\n"
  "v_max_f32 v8, v8, v9\n"
  "v_max_f32 v9, v8, v9\n"
  "v_smfmac_f32_16x16x64_bf16 v[112:115], v[48:51], v[224:231], v0\n"
  "buffer_load_dwordx4 v1, s[36:39], s40 offen offset:1024 sc1 lds\n"
  "v_permlane16_swap_b32 v8, v9\n"
  "v_max_f32 v8, v8, v9\n"
  "v_mul_f32 v8, 0xbe0293ee, v8\n"
  "v_smfmac_f32_16x16x64_bf16 v[116:119], v[52:55], v[248:255], v0\n"
  "buffer_load_dword v9, v3, s[36:39], s62 offen sc1\n"
  "v_smfmac_f32_16x16x64_bf16 v[112:115], v[52:55], v[240:247], v0\n"
  "ds_read_b128 v[92:95], v17 offset:37888\n"
  "v_fma_f32 v104, v104, s60, v8\n"
  "v_fma_f32 v105, v105, s60, v8\n"
  "v_pk_fma_f32 v[106:107], v[106:107], s[60:61], v[8:9] op_sel_hi:[1,0,0]\n"
  "v_pk_fma_f32 v[108:109], v[108:109], s[60:61], v[8:9] op_sel_hi:[1,0,0]\n"
  "v_pk_fma_f32 v[110:111], v[110:111], s[60:61], v[8:9] op_sel_hi:[1,0,0]\n"
  "v_pk_fma_f32 v[72:73], v[72:73], s[60:61], v[8:9] op_sel_hi:[1,0,0]\n"
  "v_pk_fma_f32 v[74:75], v[74:75], s[60:61], v[8:9] op_sel_hi:[1,0,0]\n"
  "v_exp_f32 v104, v104\n"
  "v_exp_f32 v105, v105\n"
  "v_exp_f32 v106, v106\n"
  "v_exp_f32 v107, v107\n"
  "v_exp_f32 v108, v108\n"
  "v_exp_f32 v109, v109\n"
  "v_exp_f32 v110, v110\n"
  "v_exp_f32 v111, v111\n"
  "v_cvt_pk_bf16_f32 v56, v104, v105\n"
  "v_cvt_pk_bf16_f32 v57, v106, v107\n"
  "v_cvt_pk_bf16_f32 v58, v108, v109\n"
  "v_cvt_pk_bf16_f32 v59, v110, v111\n"
  "v_exp_f32 v72, v72\n"
  "v_exp_f32 v73, v73\n"
  "s_waitcnt vmcnt(7)\n"
  "v_mfma_f32_16x16x32_bf16 v[68:71], v[18:21], v[56:59], 0\n"
  "s_mov_b32 vcc_lo, 0xFF00FF00\n"
  "s_mov_b32 vcc_hi, 0xFF00FF00\n"
  "v_exp_f32 v74, v74\n"
  "v_mfma_f32_16x16x32_bf16 v[104:107], v[120:123], v[56:59], 0\n"
  "v_exp_f32 v75, v75\n"
  "v_mfma_f32_16x16x32_bf16 v[108:111], v[124:127], v[56:59], 0\n"
  "v_cvt_pk_bf16_f32 v60, v72, v73\n"
  "v_cvt_pk_bf16_f32 v61, v74, v75\n"
  "v_cvt_pk_bf16_f32 v76, v112, v113\n"
  "v_cvt_pk_bf16_f32 v77, v114, v115\n"
  "v_mfma_f32_16x16x16_bf16 v[68:71], v[22:23], v[60:61], v[68:71]\n"
  "v_cvt_pk_bf16_f32 v78, v116, v117\n"
  "v_cvt_pk_bf16_f32 v79, v118, v119\n"
  "v_mfma_f32_16x16x16_bf16 v[104:107], v[76:77], v[60:61], v[104:107]\n"
  "s_cmp_eq_u32 s53, 63\n"
  "s_cbranch_scc1 LAST%=\n"
  "v_mfma_f32_16x16x16_bf16 v[108:111], v[78:79], v[60:61], v[108:111]\n"
  "v_mov_b32 v112, 0\n"
  "v_mov_b32 v113, 0\n"
  "v_rcp_f32 v68, v68\n"
  "v_mov_b32 v114, 0\n"
  "v_mov_b32 v115, 0\n"
  "v_mov_b32 v116, 0\n"
  "v_mov_b32 v117, 0\n"
  "v_mov_b32 v118, 0\n"
  "v_cndmask_b32 v104, v104, v108, vcc\n"
  "v_cndmask_b32 v105, v105, v109, vcc\n"
  "v_cndmask_b32 v106, v106, v110, vcc\n"
  "v_cndmask_b32 v107, v107, v111, vcc\n"
  "v_pk_mul_f32 v[104:105], v[104:105], v[68:69] op_sel_hi:[1,0]\n"
  "v_pk_mul_f32 v[106:107], v[106:107], v[68:69] op_sel_hi:[1,0]\n"
  "v_cvt_pk_bf16_f32 v64, v104, v105\n"
  "v_cvt_pk_bf16_f32 v65, v106, v107\n"
  "ds_write_b64 v16, v[64:65]\n"
  "v_mov_b32 v119, 0\n"
  "v_mov_b32 v104, 0\n"
  "v_mov_b32 v105, 0\n"
  "v_mov_b32 v106, 0\n"
  "v_mov_b32 v107, 0\n"
  "v_mov_b32 v108, 0\n"
  "v_mov_b32 v109, 0\n"
  "v_mov_b32 v110, 0\n"
  "v_mov_b32 v111, 0\n"
  "ds_read_b128 v[96:99], v17 offset:34816\n"
  "ds_read_b128 v[100:103], v17 offset:35840\n"
  "ds_read_b128 v[24:27], v17 offset:38912\n"
  "ds_read_b128 v[28:31], v17 offset:39936\n"
  "s_waitcnt lgkmcnt(4)\n"
  "s_barrier\n"
  "ds_read_b128 v[56:59], v12 offset:20992\n"
  "ds_read_b128 v[60:63], v10 offset:20992\n"
  "ds_read_b128 v[64:67], v12 offset:22016\n"
  "ds_read_b128 v[68:71], v10 offset:22016\n"
  "s_waitcnt lgkmcnt(3)\n"
  "v_smfmac_f32_16x16x64_bf16 v[112:115], v[56:59], a[0:7], v0\n"
  "ds_read_b128 v[32:35], v5 offset:32768\n"
  "v_smfmac_f32_16x16x64_bf16 v[116:119], v[56:59], a[128:135], v0\n"
  "ds_read_b128 v[36:39], v5 offset:33792\n"
  "s_waitcnt lgkmcnt(4)\n"
  "v_smfmac_f32_16x16x64_bf16 v[112:115], v[60:63], a[16:23], v0\n"
  "ds_read_b128 v[40:43], v5 offset:36864\n"
  "v_smfmac_f32_16x16x64_bf16 v[116:119], v[60:63], a[144:151], v0\n"
  "ds_read_b128 v[44:47], v5 offset:37888\n"
  "s_waitcnt lgkmcnt(5)\n"
  "v_smfmac_f32_16x16x64_bf16 v[112:115], v[64:67], a[32:39], v0\n"
  "ds_read_b128 v[48:51], v5 offset:34816\n"
  "v_smfmac_f32_16x16x64_bf16 v[116:119], v[64:67], a[160:167], v0\n"
  "ds_read_b128 v[52:55], v5 offset:35840\n"
  "s_waitcnt lgkmcnt(6)\n"
  "v_smfmac_f32_16x16x64_bf16 v[112:115], v[68:71], a[48:55], v0\n"
  "ds_read_b128 v[120:123], v5 offset:38912\n"
  "v_smfmac_f32_16x16x64_bf16 v[116:119], v[68:71], a[176:183], v0\n"
  "ds_read_b128 v[124:127], v5 offset:39936\n"
  "v_smfmac_f32_16x16x64_bf16 v[104:107], v[56:59], v[80:87], v0\n"
  "s_nop 1\n"
  "v_smfmac_f32_16x16x64_bf16 v[108:111], v[56:59], v[88:95], v0\n"
  "s_nop 1\n"
  "v_smfmac_f32_16x16x64_bf16 v[104:107], v[60:63], v[96:103], v0\n"
  "v_cvt_pk_bf16_f32 v72, v112, v116\n"
  "v_smfmac_f32_16x16x64_bf16 v[108:111], v[60:63], v[24:31], v0\n"
  "v_cvt_pk_bf16_f32 v73, v113, v117\n"
  "s_waitcnt lgkmcnt(6)\n"
  "v_smfmac_f32_16x16x64_bf16 v[104:107], v[64:67], v[32:39], v0\n"
  "v_cvt_pk_bf16_f32 v74, v114, v118\n"
  "s_waitcnt lgkmcnt(4)\n"
  "v_smfmac_f32_16x16x64_bf16 v[108:111], v[64:67], v[40:47], v0\n"
  "v_cvt_pk_bf16_f32 v75, v115, v119\n"
  "s_waitcnt lgkmcnt(2)\n"
  "v_smfmac_f32_16x16x64_bf16 v[104:107], v[68:71], v[48:55], v0\n"
  "buffer_store_dwordx4 v[72:75], v2, s[36:39], s55 offen sc1\n"
  "s_waitcnt lgkmcnt(0)\n"
  "v_smfmac_f32_16x16x64_bf16 v[108:111], v[68:71], v[120:127], v0\n"
  "s_add_u32 s58, s55, 65536\n"
  "s_nop 1\n"
  "v_mov_b32 v4, v4\n"
  "v_mov_b32 v4, v4\n"
  "v_mov_b32 v4, v4\n"
  "v_mov_b32 v4, v4\n"
  "v_mov_b32 v4, v4\n"
  "v_cvt_pk_bf16_f32 v84, v104, v108\n"
  "v_cvt_pk_bf16_f32 v85, v105, v109\n"
  "v_cvt_pk_bf16_f32 v86, v106, v110\n"
  "v_cvt_pk_bf16_f32 v87, v107, v111\n"
  "buffer_store_dwordx4 v[84:87], v2, s[36:39], s58 offen sc1\n"
  "s_add_u32 s53, s53, 1\n"
  "s_mov_b32 s59, 0\n"
  "s_waitcnt vmcnt(2)\n"
  "FCHK%=:\n"
  "v_subrev_u32 v8, s94, v9\n"
  "v_xor_b32 v24, s94, v9\n"
  "v_bfe_i32 v8, v8, 0, 24\n"
  "v_cmp_gt_u32 vcc, 0x1000000, v24\n"
  "v_readlane_b32 s96, v9, 32\n"
  "v_max_i32_dpp v8, v8, v8 row_ror:8 row_mask:0xf bank_mask:0xf\n"
  "v_readlane_b32 s97, v9, 33\n"
  "s_cmp_eq_u32 vcc_lo, -1\n"
  "v_max_i32_dpp v8, v8, v8 row_ror:4 row_mask:0xf bank_mask:0xf\n"
  "s_cbranch_scc0 FLAGF%=\n"
  "FGO%=:\n"
  "s_add_u32 s42, s53, 1\n"
  "v_max_i32_dpp v8, v8, v8 row_ror:2 row_mask:0xf bank_mask:0xf\n"
  "s_min_u32 s42, s42, 63\n"
  "s_lshr_b32 s42, s42, 1\n"
  "v_max_i32_dpp v8, v8, v8 row_ror:1 row_mask:0xf bank_mask:0xf\n"
  "s_and_b32 s96, s96, s97\n"
  "s_bitcmp1_b32 s96, s42\n"
  "v_readlane_b32 s96, v8, 0\n"
  "v_readlane_b32 s97, v8, 16\n"
  "s_cbranch_scc0 KVF%=\n"
  "s_add_u32 s46, s44, s50\n"
  "s_max_i32 s96, s96, s97\n"
  "s_min_i32 s96, s96, 40000\n"
  "s_add_u32 s46, s46, s96\n"
  "D1_c%=:\n"
  "s_memtime s[96:97]\n"
  "s_getpc_b64 s[40:41]\n"
  "D1_pc%=:\n"
  "s_waitcnt lgkmcnt(0)\n"
  "s_sub_i32 s96, s46, s96\n"
  "s_cmp_gt_i32 s96, 448\n"
  "s_cbranch_scc1 D1_c%=\n"
  "s_max_i32 s96, s96, 0\n"
  "s_and_b32 s96, s96, 0x1fc\n"
  "s_sub_u32 s96, D1_e%=-D1_pc%=, s96\n"
  "s_add_u32 s40, s40, s96\n"
  "s_addc_u32 s41, s41, 0\n"
  "s_setpc_b64 s[40:41]\n"
  "s_nop 0\n"
  "s_nop 0\n"
  "s_nop 0\n"
  "s_nop 0\n"
  "s_nop 0\n"
  "s_nop 0\n"
  "s_nop 0\n"
  "s_nop 0\n"
  "s_nop 0\n"
  "s_nop 0\n"
  "s_nop 0\n"
  "s_nop 0\n"
  "s_nop 0\n"
  "s_nop 0\n"
  "s_nop 0\n"
  "s_nop 0\n"
  "s_nop 0\n"
  "s_nop 0\n"
  "s_nop 0\n"
  "s_nop 0\n"
  "s_nop 0\n"
  "s_nop 0\n"
  "s_nop 0\n"
  "s_nop 0\n"
  "s_nop 0\n"
  "s_nop 0\n"
  "s_nop 0\n"
  "s_nop 0\n"
  "s_nop 0\n"
  "s_nop 0\n"
  "s_nop 0\n"
  "s_nop 0\n"
  "s_nop 0\n"
  "s_nop 0\n"
  "s_nop 0\n"
  "s_nop 0\n"
  "s_nop 0\n"
  "s_nop 0\n"
  "s_nop 0\n"
  "s_nop 0\n"
  "s_nop 0\n"
  "s_nop 0\n"
  "s_nop 0\n"
  "s_nop 0\n"
  "s_nop 0\n"
  "s_nop 0\n"
  "s_nop 0\n"
  "s_nop 0\n"
  "s_nop 0\n"
  "s_nop 0\n"
  "s_nop 0\n"
  "s_nop 0\n"
  "s_nop 0\n"
  "s_nop 0\n"
  "s_nop 0\n"
  "s_nop 0\n"
  "s_nop 0\n"
  "s_nop 0\n"
  "s_nop 0\n"
  "s_nop 0\n"
  "s_nop 0\n"
  "s_nop 0\n"
  "s_nop 0\n"
  "s_nop 0\n"
  "s_nop 0\n"
  "s_nop 0\n"
  "s_nop 0\n"
  "s_nop 0\n"
  "s_nop 0\n"
  "s_nop 0\n"
  "s_nop 0\n"
  "s_nop 0\n"
  "s_nop 0\n"
  "s_nop 0\n"
  "s_nop 0\n"
  "s_nop 0\n"
  "s_nop 0\n"
  "s_nop 0\n"
  "s_nop 0\n"
  "s_nop 0\n"
  "s_nop 0\n"
  "s_nop 0\n"
  "s_nop 0\n"
  "s_nop 0\n"
  "s_nop 0\n"
  "s_nop 0\n"
  "s_nop 0\n"
  "s_nop 0\n"
  "s_nop 0\n"
  "s_nop 0\n"
  "s_nop 0\n"
  "s_nop 0\n"
  "s_nop 0\n"
  "s_nop 0\n"
  "s_nop 0\n"
  "s_nop 0\n"
  "s_nop 0\n"
  "s_nop 0\n"
  "s_nop 0\n"
  "s_nop 0\n"
  "s_nop 0\n"
  "s_nop 0\n"
  "s_nop 0\n"
  "s_nop 0\n"
  "s_nop 0\n"
  "s_nop 0\n"
  "s_nop 0\n"
  "s_nop 0\n"
  "s_nop 0\n"
  "s_nop 0\n"
  "s_nop 0\n"
  "s_nop 0\n"
  "s_nop 0\n"
  "D1_e%=:\n"
  "buffer_load_dwordx4 v[24:27], v1, s[36:39], s61 offen sc1\n"
  "buffer_load_dwordx4 v[28:31], v1, s[36:39], s61 offen offset:1024 sc1\n"
  "s_memrealtime s[96:97]\n"
  "s_memtime s[44:45]\n"
  "s_lshl_b32 s54, s53, 14\n"
  "s_add_u32 s54, s54, s98\n"
  "s_add_u32 s56, s56, 16384\n"
  "s_add_u32 s57, s57, 16384\n"
  "v_mov_b32 v112, v76\n"
  "v_mov_b32 v113, v77\n"
  "v_mov_b32 v114, v78\n"
  "v_mov_b32 v115, v79\n"
  "ds_read_b128 v[32:35], v12 offset:25600\n"
  "ds_read_b32 v8, v12 offset:26624\n"
  "ds_read_b128 v[40:43], v17 offset:0\n"
  "ds_read_b128 v[44:47], v17 offset:1024\n"
  "ds_read_b128 v[48:51], v17 offset:2048\n"
  "ds_read_b128 v[52:55], v17 offset:3072\n"
  "ds_read_b128 v[56:59], v17 offset:4096\n"
  "ds_read_b128 v[60:63], v17 offset:5120\n"
  "ds_read_b128 v[64:67], v17 offset:6144\n"
  "ds_read_b128 v[68:71], v17 offset:7168\n"
  "ds_read_b128 v[72:75], v5 offset:0\n"
  "ds_read_b128 v[76:79], v5 offset:1024\n"
  "v_mov_b32 v36, 0\n"
  "v_mov_b32 v37, 0\n"
  "v_mov_b32 v38, 0\n"
  "v_mov_b32 v39, 0\n"
  "v_mov_b32 v104, 0\n"
  "v_mov_b32 v105, 0\n"
  "v_mov_b32 v106, 0\n"
  "v_mov_b32 v107, 0\n"
  "v_mov_b32 v108, 0\n"
  "v_mov_b32 v109, 0\n"
  "v_mov_b32 v110, 0\n"
  "v_mov_b32 v111, 0\n"
  "v_mov_b32 v116, 0\n"
  "v_mov_b32 v117, 0\n"
  "v_mov_b32 v118, 0\n"
  "v_mov_b32 v119, 0\n"
  "s_mov_b32 s59, 0\n"
  "s_waitcnt lgkmcnt(0)\n"
  "s_mul_i32 s93, s96, 23\n"
  "s_sub_u32 s93, s93, s44\n"
  "s_waitcnt vmcnt(0)\n"
  "RED1%=:\n"
  "v_smfmac_f32_16x16x64_bf16 v[36:39], v[32:35], v[24:31], v8\n"
  "s_nop 7\n"
  "v_add_f32_dpp v38, v36, v36 row_ror:4 row_mask:0xf bank_mask:0xf\n"
  "v_add_f32_dpp v39, v37, v37 row_ror:4 row_mask:0xf bank_mask:0xf\n"
  "s_nop 0\n"
  "v_add_f32_dpp v36, v38, v38 row_ror:8 row_mask:0xf bank_mask:0xf\n"
  "v_add_f32_dpp v37, v39, v39 row_ror:8 row_mask:0xf bank_mask:0xf\n"
  "v_cmp_u_f32 vcc, v36, v37\n"
  "v_cvt_pk_bf16_f32 v38, v36, v37\n"
  "s_cbranch_vccnz SLOW1%=\n"
  "CONT1%=:\n"
  "buffer_store_dword v38, v4, s[36:39], s54 offen sc1\n"
  "s_xor_b32 s55, s55, 524288\n"
  "s_xor_b32 s61, s61, 524288\n"
  "s_nop 0\n"
  "s_branch LOOP%=\n"
  "SLOWB%=:\n"
  "v_mov_b32 v76, 0\n"
  "ds_read_b128 v[72:75], v12 offset:0\n"
  "s_waitcnt lgkmcnt(0)\n"
  "v_max3_u32 v76, v76, v72, v73\n"
  "v_max3_u32 v76, v76, v74, v75\n"
  "ds_read_b128 v[72:75], v12 offset:1024\n"
  "s_waitcnt lgkmcnt(0)\n"
  "v_max3_u32 v76, v76, v72, v73\n"
  "v_max3_u32 v76, v76, v74, v75\n"
  "ds_read_b128 v[72:75], v12 offset:2048\n"
  "s_waitcnt lgkmcnt(0)\n"
  "v_max3_u32 v76, v76, v72, v73\n"
  "v_max3_u32 v76, v76, v74, v75\n"
  "ds_read_b128 v[72:75], v12 offset:3072\n"
  "s_waitcnt lgkmcnt(0)\n"
  "v_max3_u32 v76, v76, v72, v73\n"
  "v_max3_u32 v76, v76, v74, v75\n"
  "ds_read_b128 v[72:75], v12 offset:4096\n"
  "s_waitcnt lgkmcnt(0)\n"
  "v_max3_u32 v76, v76, v72, v73\n"
  "v_max3_u32 v76, v76, v74, v75\n"
  "ds_read_b128 v[72:75], v12 offset:5120\n"
  "s_waitcnt lgkmcnt(0)\n"
  "v_max3_u32 v76, v76, v72, v73\n"
  "v_max3_u32 v76, v76, v74, v75\n"
  "ds_read_b128 v[72:75], v12 offset:6144\n"
  "s_waitcnt lgkmcnt(0)\n"
  "v_max3_u32 v76, v76, v72, v73\n"
  "v_max3_u32 v76, v76, v74, v75\n"
  "ds_read_b128 v[72:75], v12 offset:7168\n"
  "s_waitcnt lgkmcnt(0)\n"
  "v_max3_u32 v76, v76, v72, v73\n"
  "v_max3_u32 v76, v76, v74, v75\n"
  "v_cmp_eq_u32 vcc, s52, v76\n"
  "s_nop 1\n"
  "s_cbranch_vccz CONTB%=\n"
  "RETRY2%=:\n"
  "s_waitcnt lgkmcnt(0)\n"
  "s_barrier\n"
  "s_mov_b32 m0, s65\n"
  "s_nop 0\n"
  "buffer_load_dwordx4 v1, s[36:39], s54 offen sc1 lds\n"
  "buffer_load_dwordx4 v1, s[36:39], s54 offen offset:1024 sc1 lds\n"
  "s_nop 7\n"
  "ds_read_b128 v[40:43], v17 offset:0\n"
  "ds_read_b128 v[44:47], v17 offset:1024\n"
  "ds_read_b128 v[48:51], v17 offset:2048\n"
  "ds_read_b128 v[52:55], v17 offset:3072\n"
  "ds_read_b128 v[56:59], v17 offset:4096\n"
  "ds_read_b128 v[60:63], v17 offset:5120\n"
  "ds_read_b128 v[64:67], v17 offset:6144\n"
  "ds_read_b128 v[68:71], v17 offset:7168\n"
  "ds_read_b128 v[72:75], v5 offset:0\n"
  "ds_read_b128 v[76:79], v5 offset:1024\n"
  "ds_read_b128 v[80:83], v5 offset:2048\n"
  "ds_read_b128 v[84:87], v5 offset:3072\n"
  "ds_read_b128 v[88:91], v5 offset:4096\n"
  "ds_read_b128 v[92:95], v5 offset:5120\n"
  "ds_read_b128 v[96:99], v5 offset:6144\n"
  "ds_read_b128 v[100:103], v5 offset:7168\n"
  "v_mov_b32 v104, 0\n"
  "v_mov_b32 v105, 0\n"
  "v_mov_b32 v106, 0\n"
  "v_mov_b32 v107, 0\n"
  "v_mov_b32 v108, 0\n"
  "v_mov_b32 v109, 0\n"
  "v_mov_b32 v110, 0\n"
  "v_mov_b32 v111, 0\n"
  "v_mov_b32 v116, 0\n"
  "v_mov_b32 v117, 0\n"
  "v_mov_b32 v118, 0\n"
  "v_mov_b32 v119, 0\n"
  "v_mov_b32 v6, s52\n"
  "v_mov_b32 v7, s52\n"
  "v_mov_b32 v8, s52\n"
  "v_mov_b32 v9, s52\n"
  "s_mov_b32 s41, 0x7FFF0000\n"
  "s_add_u32 s64, s64, 1000\n"
  "s_add_u32 s59, s59, 1\n"
  "s_cmp_lt_u32 s59, 0x8000\n"
  "s_waitcnt vmcnt(0) lgkmcnt(0)\n"
  "s_cbranch_scc1 T0%=\n"
  "s_branch END%=\n"
  "SLOW1%=:\n"
  "v_max3_u32 v39, v24, v25, v26\n"
  "v_max3_u32 v39, v39, v27, v28\n"
  "v_max3_u32 v39, v39, v29, v30\n"
  "v_max_u32 v39, v39, v31\n"
  "v_cmp_eq_u32 vcc, s52, v39\n"
  "s_cbranch_vccz CONT1%=\n"
  "buffer_load_dwordx4 v[24:27], v1, s[36:39], s61 offen sc1\n"
  "buffer_load_dwordx4 v[28:31], v1, s[36:39], s61 offen offset:1024 sc1\n"
  "v_mov_b32 v36, 0\n"
  "v_mov_b32 v37, 0\n"
  "v_mov_b32 v38, 0\n"
  "v_mov_b32 v39, 0\n"
  "s_add_u32 s63, s63, 1\n"
  "s_add_u32 s59, s59, 1\n"
  "s_cmp_lt_u32 s59, 0x8000\n"
  "s_waitcnt vmcnt(0)\n"
  "s_cbranch_scc1 RED1%=\n"
  "s_branch END%=\n"
  "CANF%=:\n"
  "s_waitcnt lgkmcnt(0)\n"
  "s_barrier\n"
  "s_mov_b32 m0, s65\n"
  "s_nop 0\n"
  "buffer_load_dwordx4 v1, s[36:39], s54 offen sc1 lds\n"
  "buffer_load_dwordx4 v1, s[36:39], s54 offen offset:1024 sc1 lds\n"
  "s_nop 7\n"
  "v_mov_b32 v104, 0\n"
  "v_mov_b32 v105, 0\n"
  "v_mov_b32 v106, 0\n"
  "v_mov_b32 v107, 0\n"
  "v_mov_b32 v108, 0\n"
  "v_mov_b32 v109, 0\n"
  "v_mov_b32 v110, 0\n"
  "v_mov_b32 v111, 0\n"
  "s_add_u32 s64, s64, 1\n"
  "s_add_u32 s59, s59, 1\n"
  "s_cmp_lt_u32 s59, 0x8000\n"
  "s_waitcnt vmcnt(0)\n"
  "s_cbranch_scc1 T0%=\n"
  "s_branch END%=\n"
  "FLAGF%=:\n"
  "buffer_load_dword v9, v3, s[36:39], s62 offen sc1\n"
  "s_add_u32 s63, s63, 1000\n"
  "s_add_u32 s59, s59, 1\n"
  "s_cmp_lt_u32 s59, 0x8000\n"
  "s_waitcnt vmcnt(0)\n"
  "s_cbranch_scc1 FCHK%=\n"
  "s_mov_b32 s59, 0\n"
  "KVF%=:\n"
  "s_sleep 2\n"
  "buffer_load_dword v9, v3, s[36:39], s62 offen sc1\n"
  "v_mov_b32 v8, 0\n"
  "s_add_u32 s59, s59, 1\n"
  "s_cmp_lt_u32 s59, 0x20000\n"
  "s_waitcnt vmcnt(0)\n"
  "v_readlane_b32 s96, v9, 32\n"
  "v_readlane_b32 s97, v9, 33\n"
  "s_cbranch_scc1 FGO%=\n"
  "s_branch END%=\n"
  "LAST%=:\n"
  "buffer_store_dwordx4 v[76:79], v1, s[36:39], s57 offen offset:1024 sc1\n"
  "END%=:\n"
  "s_nop 7\n"
  "s_waitcnt vmcnt(0) lgkmcnt(0)\n"
    :
    : "s"(ws), "s"(pq), "s"(pk), "s"(pv), "s"(po), "s"(w), "s"(pd1), "s"(pd2), "s"(h)
    :
    "v0", "v1", "v2", "v3", "v4", "v5", "v6", "v7", "v8", "v9", "v10", "v11", "v12", "v13", "v14", "v15", "v16", "v17", "v18", "v19", "v20", "v21", "v22", "v23",
    "v24", "v25", "v26", "v27", "v28", "v29", "v30", "v31", "v32", "v33", "v34", "v35", "v36", "v37", "v38", "v39", "v40", "v41", "v42", "v43", "v44", "v45", "v46", "v47",
    "v48", "v49", "v50", "v51", "v52", "v53", "v54", "v55", "v56", "v57", "v58", "v59", "v60", "v61", "v62", "v63", "v64", "v65", "v66", "v67", "v68", "v69", "v70", "v71",
    "v72", "v73", "v74", "v75", "v76", "v77", "v78", "v79", "v80", "v81", "v82", "v83", "v84", "v85", "v86", "v87", "v88", "v89", "v90", "v91", "v92", "v93", "v94", "v95",
    "v96", "v97", "v98", "v99", "v100", "v101", "v102", "v103", "v104", "v105", "v106", "v107", "v108", "v109", "v110", "v111", "v112", "v113", "v114", "v115", "v116", "v117", "v118", "v119",
    "v120", "v121", "v122", "v123", "v124", "v125", "v126", "v127", "v128", "v129", "v130", "v131", "v132", "v133", "v134", "v135", "v136", "v137", "v138", "v139", "v140", "v141", "v142", "v143",
    "v144", "v145", "v146", "v147", "v148", "v149", "v150", "v151", "v152", "v153", "v154", "v155", "v156", "v157", "v158", "v159", "v160", "v161", "v162", "v163", "v164", "v165", "v166", "v167",
    "v168", "v169", "v170", "v171", "v172", "v173", "v174", "v175", "v176", "v177", "v178", "v179", "v180", "v181", "v182", "v183", "v184", "v185", "v186", "v187", "v188", "v189", "v190", "v191",
    "v192", "v193", "v194", "v195", "v196", "v197", "v198", "v199", "v200", "v201", "v202", "v203", "v204", "v205", "v206", "v207", "v208", "v209", "v210", "v211", "v212", "v213", "v214", "v215",
    "v216", "v217", "v218", "v219", "v220", "v221", "v222", "v223", "v224", "v225", "v226", "v227", "v228", "v229", "v230", "v231", "v232", "v233", "v234", "v235", "v236", "v237", "v238", "v239",
    "v240", "v241", "v242", "v243", "v244", "v245", "v246", "v247", "v248", "v249", "v250", "v251", "v252", "v253", "v254", "v255", "a0", "a1", "a2", "a3", "a4", "a5", "a6", "a7",
    "a8", "a9", "a10", "a11", "a12", "a13", "a14", "a15", "a16", "a17", "a18", "a19", "a20", "a21", "a22", "a23", "a24", "a25", "a26", "a27", "a28", "a29", "a30", "a31",
    "a32", "a33", "a34", "a35", "a36", "a37", "a38", "a39", "a40", "a41", "a42", "a43", "a44", "a45", "a46", "a47", "a48", "a49", "a50", "a51", "a52", "a53", "a54", "a55",
    "a56", "a57", "a58", "a59", "a60", "a61", "a62", "a63", "a64", "a65", "a66", "a67", "a68", "a69", "a70", "a71", "a72", "a73", "a74", "a75", "a76", "a77", "a78", "a79",
    "a80", "a81", "a82", "a83", "a84", "a85", "a86", "a87", "a88", "a89", "a90", "a91", "a92", "a93", "a94", "a95", "a96", "a97", "a98", "a99", "a100", "a101", "a102", "a103",
    "a104", "a105", "a106", "a107", "a108", "a109", "a110", "a111", "a112", "a113", "a114", "a115", "a116", "a117", "a118", "a119", "a120", "a121", "a122", "a123", "a124", "a125", "a126", "a127",
    "a128", "a129", "a130", "a131", "a132", "a133", "a134", "a135", "a136", "a137", "a138", "a139", "a140", "a141", "a142", "a143", "a144", "a145", "a146", "a147", "a148", "a149", "a150", "a151",
    "a152", "a153", "a154", "a155", "a156", "a157", "a158", "a159", "a160", "a161", "a162", "a163", "a164", "a165", "a166", "a167", "a168", "a169", "a170", "a171", "a172", "a173", "a174", "a175",
    "a176", "a177", "a178", "a179", "a180", "a181", "a182", "a183", "a184", "a185", "a186", "a187", "a188", "a189", "a190", "a191", "a192", "a193", "a194", "a195", "a196", "a197", "a198", "a199",
    "a200", "a201", "a202", "a203", "a204", "a205", "a206", "a207", "a208", "a209", "a210", "a211", "a212", "a213", "a214", "a215", "a216", "a217", "a218", "a219", "a220", "a221", "a222", "a223",
    "a224", "a225", "a226", "a227", "a228", "a229", "a230", "a231", "a232", "a233", "a234", "a235", "a236", "a237", "a238", "a239", "a240", "a241", "a242", "a243", "a244", "a245", "a246", "a247",
    "a248", "a249", "a250", "a251", "a252", "a253", "a254", "a255", "s36", "s37", "s38", "s39", "s40", "s41", "s42", "s43", "s44", "s45", "s46", "s47", "s48", "s49", "s50", "s51",
    "s52", "s53", "s54", "s55", "s56", "s57", "s58", "s59", "s60", "s61", "s62", "s63", "s64", "s65", "s66", "s67", "s68", "s69", "s70", "s71", "s72", "s73", "s74", "s75",
    "s76", "s77", "s78", "s79", "s80", "s81", "s82", "s83", "s84", "s85", "s86", "s87", "s88", "s89", "s90", "s91", "s92", "s93", "s94", "s95", "s96", "s97", "s98", "s99",
    "vcc", "scc", "memory");
}

typedef __amdgpu_buffer_rsrc_t rsrc_t;
DEVI u32x4 ld128s(rsrc_t r, unsigned off) { return __builtin_amdgcn_raw_buffer_load_b128(r, off, 0, 16); }
DEVI u32x2 ld64s(rsrc_t r, unsigned off) { return __builtin_amdgcn_raw_buffer_load_b64(r, off, 0, 16); }
DEVI unsigned ld32s(rsrc_t r, unsigned off) { return __builtin_amdgcn_raw_buffer_load_b32(r, off, 0, 16); }
DEVI void st128s(rsrc_t r, unsigned off, u32x4 v) { __builtin_amdgcn_raw_buffer_store_b128(v, r, off, 0, 16); }
DEVI unsigned max4(u32x4 a) { return max(max(a[0], a[1]), max(a[2], a[3])); }
#define MEMFENCE_C() asm volatile("" ::: "memory")
constexpr int SPIN_CAP = 1 << 17;
#define CD_LAST_SLEEP 10
template <class P> DEVI void wait_word(rsrc_t r, unsigned off, bool& dead, int slp, P pred) {
  for (int i = 0; !dead; ++i) {
    MEMFENCE_C();
    const unsigned v = __builtin_amdgcn_readfirstlane(ld32s(r, off));
    if (pred(v)) break;
    if (i > SPIN_CAP) dead = true;
    for (int k = 0; k < slp; ++k) __builtin_amdgcn_s_sleep(4);
  }
  MEMFENCE_C();
}
DEVI void arrive(unsigned char* ws, size_t cnt, size_t flag, unsigned n, int l) {
  unsigned old = 0;
  if (l == 0) old = __hip_atomic_fetch_add((GAS unsigned*)(ws + WS_SYNC + cnt), 1u, __ATOMIC_RELAXED, __HIP_MEMORY_SCOPE_AGENT);
  if (__builtin_amdgcn_readfirstlane(old) == n - 1 && l < 4) __hip_atomic_store((GAS unsigned*)(ws + WS_SYNC + flag + l * 64), 1u, __ATOMIC_RELAXED, __HIP_MEMORY_SCOPE_AGENT);
}
DEVI void wait_flag(rsrc_t r, size_t flag, bool& dead, int slp) { wait_word(r, (unsigned)(WS_SYNC + flag) + (blockIdx.x & 3) * 64, dead, slp, [](unsigned v) { return v != 0; }); }
DEVI void raise_hint(unsigned char* ws, int i, int l) {
  if (l < 4) __hip_atomic_store((GAS unsigned*)(ws + WS_SYNC + SY_HINT + i * 256 + l * 64), 1u, __ATOMIC_RELAXED, __HIP_MEMORY_SCOPE_AGENT);
}
DEVI void wait_step_hint(rsrc_t r, unsigned flbase, int h, int step, bool& dead, int slp) {
  wait_word(r, flbase + (4 * h + (step & 3)) * 64, dead, slp, [&](unsigned v) { const int t = v >> 24; return t >= step && t < 64; });
}

DEVI void ln1_tile(int tt, const u16* x, unsigned char* ws, rsrc_t R) {
  const int tid = threadIdx.x, w = tid >> 6, l = tid & 63, r = l & 15, g = l >> 4;
  LAS float* red = (LAS float*)0;
  const float* mod = (const float*)(ws + WS_MOD);
  const u16* xr = x + (size_t)(tt * 16 + r) * 512;
  float v[4][8];
  f32x4 sa[4][2], ca[4][2];
#pragma unroll
  for (int k = 0; k < 4; ++k) {
    const int d0 = (w * 4 + k) * 32 + 4 * g;
    u32x2 a = *(const u32x2*)(xr + d0), b = *(const u32x2*)(xr + d0 + 16);
    sa[k][0] = *(const f32x4*)(mod + d0); sa[k][1] = *(const f32x4*)(mod + d0 + 16);
    ca[k][0] = *(const f32x4*)(mod + 512 + d0); ca[k][1] = *(const f32x4*)(mod + 512 + d0 + 16);
    v[k][0] = bflo(a[0]); v[k][1] = bfhi(a[0]); v[k][2] = bflo(a[1]); v[k][3] = bfhi(a[1]);
    v[k][4] = bflo(b[0]); v[k][5] = bfhi(b[0]); v[k][6] = bflo(b[1]); v[k][7] = bfhi(b[1]);
  }
  float s = 0;
#pragma unroll
  for (int k = 0; k < 4; ++k)
#pragma unroll
    for (int j = 0; j < 8; ++j) s += v[k][j];
  s = xsum32(xsum16(s));
  if (g == 0) red[w * 16 + r] = s;
  RAW_BARRIER();
  const float mean = (red[r] + red[16 + r] + red[32 + r] + red[48 + r]) * (1.f / 512.f);
  float q = 0;
#pragma unroll
  for (int k = 0; k < 4; ++k)
#pragma unroll
    for (int j = 0; j < 8; ++j) { v[k][j] -= mean; q += v[k][j] * v[k][j]; }
  q = xsum32(xsum16(q));
  if (g == 0) red[64 + w * 16 + r] = q;
  RAW_BARRIER();
  const float rs = rsqrtf((red[64 + r] + red[80 + r] + red[96 + r] + red[112 + r]) * (1.f / 512.f) + 1e-5f);
#pragma unroll
  for (int k = 0; k < 4; ++k) {
    float o[8];
#pragma unroll
    for (int j = 0; j < 8; ++j) o[j] = v[k][j] * rs * rbf(1.f + ca[k][j >> 2][j & 3]) + sa[k][j >> 2][j & 3];
    st128s(R, (unsigned)WS_HF + ((tt * 16 + w * 4 + k) * 64 + l) * 16, (u32x4){pk2(o[0], o[1]), pk2(o[2], o[3]), pk2(o[4], o[5]), pk2(o[6], o[7])});
  }
  wait_vm<0>();
  RAW_BARRIER();
  if (tid < 64) arrive(ws, SY_H + (tt >> 3) * 64, SY_H_F + (tt >> 3) * 256, 8, l);
}
DEVI unsigned get_flbase(rsrc_t R, bool& dead) {
  unsigned p = 1;
  wait_word(R, (unsigned)(WS_SYNC + SY_POL), dead, 2, [&](unsigned v) { p = v; return v != 0; });
  return (unsigned)MB + (((p - 1) & 1) << 13);
}

typedef Gemm<8, 256, 2, 2, 2, true, 3, true> GqkT;
typedef Gemm<8, 256, 2, 2, 2, false, 3, true> GvT;
DEVI void qkv_unit(int mat, int nb, int mp, const u16* Wq, const u16* Wk, const u16* Wv, unsigned char* ws, rsrc_t R) {
  const int w = __builtin_amdgcn_readfirstlane(threadIdx.x >> 6), l = threadIdx.x & 63, wm = w >> 1, wn = w & 1;
  f32x4 acc[4][8];
#pragma unroll
  for (int m = 0; m < 4; ++m)
#pragma unroll
    for (int n = 0; n < 8; ++n) acc[m][n] = (f32x4){0, 0, 0, 0};
  const unsigned rec = ((nb * 2 + wn) * 64 + mp * 4 + wm * 2) * 16384 + l * 16;
  auto pre = [&]() { bool dead = false; wait_flag(R, SY_H_F + mp * 256, dead, 1); };
  if (mat != 1) {
    GqkT::run(acc, ws + WS_HF, 16, mp * 8, 0, mat ? Wq : Wk, HE, nb * 256, 8, 0, pre);
    const unsigned dst = (unsigned)(mat ? WS_QM : WS_KV) + rec;
#pragma unroll
    for (int m = 0; m < 4; ++m)
#pragma unroll
      for (int ks = 0; ks < 4; ++ks) {
        const f32x4 a = acc[m][2 * ks], c = acc[m][2 * ks + 1];
        st128s(R, dst + (m >> 1) * 16384 + ((m & 1) * 4 + ks) * 1024, (u32x4){pk2(a[0], a[1]), pk2(a[2], a[3]), pk2(c[0], c[1]), pk2(c[2], c[3])});
      }
    if (mat == 0) {
#pragma unroll
      for (int i = 0; i < 16; ++i) st128s(R, (unsigned)WS_QM + rec + (i >> 3) * 16384 + 8192 + (i & 7) * 1024, (u32x4){SENT, SENT, SENT, SENT});
    }
  } else {
    GvT::run(acc, ws + WS_HF, 16, mp * 8, 0, Wv, HE, nb * 256, 8, 0, pre);
#pragma unroll
    for (int sg = 0; sg < 2; ++sg)
#pragma unroll
      for (int et = 0; et < 8; ++et) {
        const f32x4 a = acc[2 * sg][et], c = acc[2 * sg + 1][et];
        st128s(R, (unsigned)WS_KV + 8192 + rec + sg * 16384 + et * 1024, (u32x4){pk2(a[0], a[1]), pk2(a[2], a[3]), pk2(c[0], c[1]), pk2(c[2], c[3])});
      }
  }
  wait_vm<0>();
  RAW_BARRIER();
  if (threadIdx.x < 8)
    __hip_atomic_fetch_or((GAS unsigned*)(ws + WS_SYNC + (mat * 32 + nb * 2) * 256 + threadIdx.x * 64), 3u << (2 * mp), __ATOMIC_RELAXED, __HIP_MEMORY_SCOPE_AGENT);
}

template <bool ISK>
DEVI void kv_fast_t(int h, int sp, const u16* W, unsigned char* ws, rsrc_t R) {
  const int w = __builtin_amdgcn_readfirstlane(threadIdx.x >> 6), l = threadIdx.x & 63, wm = w >> 1, wn = w & 1, g = l >> 4, q4 = (l >> 2) & 3, p4 = l & 3;
  const unsigned char* src = (const unsigned char*)(W + (size_t)w * 128 * HE + h * 128);
  const unsigned vo0 = (q4 * HE + g * 32 + (p4 >> 1) * 16 + (p4 & 1) * 8) * 2, vo1 = (vo0 ^ 32) + 4 * HE * 2;
#pragma unroll 4
  for (int i = 0; i < 16; ++i) {
    gldsS<false>(vo0, src + (size_t)i * 8 * HE * 2, w * 32768 + i * 2048);
    gldsS<false>(vo1, src + (size_t)i * 8 * HE * 2, w * 32768 + i * 2048 + 1024);
  }
  const unsigned rec = (h * 64 + sp * 2 + wm) * 16384 + l * 16;
  if (ISK) {
#pragma unroll
    for (int i = 0; i < 4; ++i) st128s(R, (unsigned)WS_QM + rec + 8192 + (wn * 4 + i) * 1024, (u32x4){SENT, SENT, SENT, SENT});
  }
    bool dead = false;
  wait_flag(R, SY_H_F + (sp >> 1) * 256, dead, 0);
  wait_vm<0>();
    u32x4 xf[2][16];
#pragma unroll
  for (int k = 0; k < 16; ++k)
#pragma unroll
    for (int m = 0; m < 2; ++m) xf[m][k] = ld128s(R, (unsigned)WS_HF + ((sp * 4 + wm * 2 + m) * 16 + k) * 1024 + l * 16);
  SCHED_FENCE();
  RAW_BARRIER();
  f32x4 acc[2][4];
#pragma unroll
  for (int m = 0; m < 2; ++m)
#pragma unroll
    for (int n = 0; n < 4; ++n) acc[m][n] = (f32x4){0, 0, 0, 0};
  const unsigned rb = g * 1024 + q4 * 64 + p4 * 8 + wn * 512;
  const unsigned rE[2] = {rb + (g & 1) * 32, rb + (g & 1) * 32 + 65536}, rO[2] = {rb + ((g & 1) ^ 1) * 32, rb + ((g & 1) ^ 1) * 32 + 65536};
  u32x4 wf[2][4];
  auto rdw = [&](int k) {
#pragma unroll
    for (int n = 0; n < 4; ++n) {
      const unsigned a = ((n & 1) ? rO[k >> 3] : rE[k >> 3]) + (k & 7) * 8192 + (n >> 1) * 256;
      const u32x2 lo = trread(a), hi = trread(a + 4096);
      wf[k & 1][n] = (u32x4){lo[0], lo[1], hi[0], hi[1]};
    }
  };
  rdw(0);
#pragma unroll
  for (int k = 0; k < 16; ++k) {
    if (k < 15) rdw(k + 1);
#pragma unroll
    for (int m = 0; m < 2; ++m)
#pragma unroll
      for (int n = 0; n < 4; ++n) acc[m][n] = ISK ? mfma32(wf[k & 1][n], xf[m][k], acc[m][n]) : mfma32(xf[m][k], wf[k & 1][n], acc[m][n]);
    SCHED_FENCE();
  }
    if (ISK) {
#pragma unroll
    for (int m = 0; m < 2; ++m)
#pragma unroll
      for (int k = 0; k < 2; ++k) {
        const f32x4 a = acc[m][2 * k], c = acc[m][2 * k + 1];
        st128s(R, (unsigned)WS_KV + rec + (m * 4 + wn * 2 + k) * 1024, (u32x4){pk2(a[0], a[1]), pk2(a[2], a[3]), pk2(c[0], c[1]), pk2(c[2], c[3])});
      }
  } else {
#pragma unroll
    for (int n = 0; n < 4; ++n) {
      const f32x4 a = acc[0][n], c = acc[1][n];
      st128s(R, (unsigned)WS_KV + 8192 + rec + (wn * 4 + n) * 1024, (u32x4){pk2(a[0], a[1]), pk2(a[2], a[3]), pk2(c[0], c[1]), pk2(c[2], c[3])});
    }
  }
  wait_vm<0>();
  RAW_BARRIER();
  if (threadIdx.x < 4)
    __hip_atomic_fetch_or((GAS unsigned*)(ws + WS_SYNC + ((ISK ? 0 : 32) + h) * 256 + threadIdx.x * 64), 1u << sp, __ATOMIC_RELAXED, __HIP_MEMORY_SCOPE_AGENT);
}
DEVI void kv_fast(int mat, int h, int sp, const u16* Wk, const u16* Wv, unsigned char* ws, rsrc_t R) {
  if (mat == 0) kv_fast_t<true>(h, sp, Wk, ws, R); else kv_fast_t<false>(h, sp, Wv, ws, R);
}

DEVI void g2_issue(int half, int nb, int w, const u16* w2);
DEVI void cd_worker(int hp, int rep, const u16* Wo, const u16* w2, int nb2, unsigned char* ws, rsrc_t R) {
  const int w = __builtin_amdgcn_readfirstlane(threadIdx.x >> 6), l = threadIdx.x & 63, c = l & 15, g = l >> 4, q4 = (l >> 2) & 3, p4 = l & 3;
  u32x4 wf[8][8];
#pragma unroll
  for (int half = 0; half < 2; ++half) {
    const unsigned char* src = (const unsigned char*)(Wo + (size_t)(hp * 256 + half * 128) * D_ + w * 128);
#pragma unroll 4
    for (int i = 0; i < 32; ++i)
      gldsS<false>(((i * 4 + q4) * D_ + g * 32 + (((p4 >> 1) ^ (i & 1)) * 16) + (p4 & 1) * 8) * 2, src, w * 32768 + i * 1024);
    wait_vm<0>();
    const unsigned rb = w * 32768 + g * 1024 + q4 * 64 + p4 * 8;
#pragma unroll
    for (int k = 0; k < 4; ++k)
#pragma unroll
      for (int n = 0; n < 8; ++n) {
        const unsigned a = rb + (((n & 1) ^ (g & 1)) * 32) + k * 8192 + (n >> 1) * 256;
        const u32x2 lo = trread(a), hi = trread(a + 4096);
        wf[half * 4 + k][n] = (u32x4){lo[0], lo[1], hi[0], hi[1]};
      }
    asm volatile("s_waitcnt lgkmcnt(0)" ::: "memory");
  }
  RAW_BARRIER();
  g2_issue(0, nb2, w, w2);
  {
    const int sl = rep == 0 ? 63 : 60 + rep;
#pragma unroll
    for (int i = 0; i < 8; ++i) st128s(R, (unsigned)WS_PP + ((sl * 32 + (i >> 2) * 16 + c) * 16 + hp) * 1024 + w * 256 + g * 16 + (i & 3) * 64, (u32x4){SENT, SENT, SENT, SENT});
  }
  const int hh = w >> 1, qt = w & 1, h = hp * 2 + hh;
  const unsigned kb = 32768 + w * 2048;
  bool dead = false;
  const unsigned flbase = get_flbase(R, dead);
  unsigned ready = 0;
  int seen = -1;
  u32x4 qf[4], kf[8], vf[8];
  auto fetch = [&](int s) {
    if (!((ready >> (s >> 1)) & 1)) {
      ready = ~0u;
#pragma unroll
      for (int m = 0; m < 3; ++m) wait_word(R, (unsigned)WS_SYNC + (m * 32 + h) * 256, dead, 4, [&](unsigned v) { const bool ok = (v >> (s >> 1)) & 1; if (ok) ready &= v; return ok; });
    }
    const unsigned rec = (h * 64 + s) * 16384 + l * 16;
#pragma unroll
    for (int i = 0; i < 4; ++i) qf[i] = ld128s(R, (unsigned)WS_QM + rec + (qt * 4 + i) * 1024);
#pragma unroll
    for (int i = 0; i < 8; ++i) kf[i] = ld128s(R, (unsigned)WS_KV + rec + i * 1024);
#pragma unroll
    for (int i = 0; i < 8; ++i) vf[i] = ld128s(R, (unsigned)WS_KV + rec + 8192 + i * 1024);
    SCHED_FENCE();
  };
  fetch(rep);
  for (int s = rep; s < NSEG; s += 3) {
    const unsigned rec = (h * 64 + s) * 16384 + l * 16;
    f32x4 sm = {0, 0, 0, 0}, s0 = {0, 0, 0, 0}, s1 = {0, 0, 0, 0};
#pragma unroll
    for (int ks = 0; ks < 4; ++ks) { s0 = mfma32(kf[ks], qf[ks], s0); s1 = mfma32(kf[4 + ks], qf[ks], s1); }
#pragma unroll
    for (int r = 0; r < 4; ++r) {
      const bool ok = (4 * g + r) <= c;
      s0[r] = (qt == 1 || ok) ? s0[r] : -INFINITY;
      s1[r] = (qt == 1 && ok) ? s1[r] : -INFINITY;
    }
    asm volatile("" : "+v"(s0), "+v"(s1));
    const int want = min(s + 1, NSEG - 1);
    if (seen < want) {
      wait_word(R, flbase + (4 * h + (want & 3)) * 64, dead, 1, [&](unsigned v) { const int tt = v >> 24; const bool ok = tt >= want && tt < 64; if (ok) seen = tt; return ok; });
      if (seen == want) { __builtin_amdgcn_s_sleep(6); if (s == NSEG - 1) __builtin_amdgcn_s_sleep(CD_LAST_SLEEP); }
    }
    u32x2 kmr[4]; u32x4 vm[4];
    for (int it = 0; !dead; ++it) {
      MEMFENCE_C();
      unsigned mx = 0;
#pragma unroll
      for (int i = 0; i < 4; ++i) {
        kmr[i] = ld64s(R, (unsigned)WS_QM + rec - l * 8 + 8192 + i * 2048);
        vm[i] = ld128s(R, (unsigned)WS_QM + rec + 8192 + i * 2048 + 1024);
      }
#pragma unroll
      for (int i = 0; i < 4; ++i) mx = max(max(mx, max4(vm[i])), max(kmr[i][0], kmr[i][1]));
      if (__all(mx != SENT)) break;
      if (it > SPIN_CAP) dead = true;
      __builtin_amdgcn_s_sleep(2);
    }
    TSTAMP(13);
#pragma unroll
    for (int i = 0; i < 4; ++i) ldsw64(kb + (32 * i + 16 * (g >> 1) + c) * 16 + (g & 1) * 8, kmr[i]);
    asm volatile("s_waitcnt lgkmcnt(0)" ::: "memory");
    u32x4 kmf[4];
#pragma unroll
    for (int i = 0; i < 4; ++i) {
      const unsigned ra = kb + (32 * i + 4 * g + q4) * 16 + (l & 1) * 8;
      const u32x2 lo = trread(ra), hi = trread(ra + 256);
      kmf[i] = (u32x4){lo[0], lo[1], hi[0], hi[1]};
    }
    SCHED_FENCE();
#pragma unroll
    for (int i = 0; i < 4; ++i) sm = mfma32(kmf[i], qf[i], sm);
    float mx = fmaxf(fmaxf(fmaxf(sm[0], sm[1]), fmaxf(sm[2], sm[3])), fmaxf(fmaxf(fmaxf(s0[0], s0[1]), fmaxf(s0[2], s0[3])), fmaxf(fmaxf(s1[0], s1[1]), fmaxf(s1[2], s1[3]))));
    mx = xmax32(xmax16(mx));
    const float nm = -mx * SM_C;
    float sum = 0, summ = 0;
#pragma unroll
    for (int r = 0; r < 4; ++r) {
      sm[r] = __builtin_amdgcn_exp2f(fmaf(sm[r], SM_C, nm)); s0[r] = __builtin_amdgcn_exp2f(fmaf(s0[r], SM_C, nm)); s1[r] = __builtin_amdgcn_exp2f(fmaf(s1[r], SM_C, nm));
      sum += s0[r] + s1[r]; summ += sm[r];
    }
    sum += (g < 2) ? summ : 0.f;
    sum = xsum32(xsum16(sum));
    const float inv = __builtin_amdgcn_rcpf(sum);
    const u32x4 Bt = {pk2(s0[0], s0[1]), pk2(s0[2], s0[3]), pk2(s1[0], s1[1]), pk2(s1[2], s1[3])};
    const u32x2 Bm = {pk2(sm[0], sm[1]), pk2(sm[2], sm[3])};
    const u32x4 BmX = {Bm[0], Bm[1], 0, 0}, BmY = {0, 0, Bm[0], Bm[1]};
    f32x4 oo[8];
#pragma unroll
    for (int e = 0; e < 8; ++e) oo[e] = mfma32(vf[e], Bt, (f32x4){0, 0, 0, 0});
#pragma unroll
    for (int e = 0; e < 8; ++e) oo[e] = mfma32(vm[e >> 1], (e & 1) ? BmY : BmX, oo[e]);
    SCHED_FENCE();
#pragma unroll
    for (int m = 0; m < 4; ++m) {
      const f32x4 oX = oo[2 * m], oY = oo[2 * m + 1];
      ldsw128((qt * 8 + hh * 4 + m) * 1024 + l * 16, (u32x4){pk2(oX[0] * inv, oX[1] * inv), pk2(oX[2] * inv, oX[3] * inv), pk2(oY[0] * inv, oY[1] * inv), pk2(oY[2] * inv, oY[3] * inv)});
    }
    RAW_BARRIER();
    TSTAMP(14);
    fetch(min(s + 3, NSEG - 1));
    u32x4 xf[16];
#pragma unroll
    for (int i = 0; i < 16; ++i) xf[i] = ldsr128(i * 1024 + l * 16);
    SCHED_FENCE();
#pragma unroll
    for (int mt = 0; mt < 2; ++mt) {
      f32x4 acc[8];
#pragma unroll
      for (int n = 0; n < 8; ++n) acc[n] = (f32x4){0, 0, 0, 0};
#pragma unroll
      for (int k = 0; k < 8; ++k) {
#pragma unroll
        for (int n = 0; n < 8; ++n) acc[n] = mfma32(wf[k][n], xf[mt * 8 + k], acc[n]);
      }
      if (mt == 1) { MFMA_DRAIN(); if (s > 60 && hp == 7 && w == 0) raise_hint(ws, s - 60, l); }
      const unsigned dst = (unsigned)WS_PP + ((s * 32 + mt * 16 + c) * 16 + hp) * 1024 + w * 256 + g * 16;
#pragma unroll
      for (int m = 0; m < 4; ++m) {
        const f32x4 a = acc[2 * m], b = acc[2 * m + 1];
        st128s(R, dst + m * 64, (u32x4){pk2(a[0], a[1]), pk2(a[2], a[3]), pk2(b[0], b[1]), pk2(b[2], b[3])});
      }
    }
    TSTAMP(15);
    if (s >= NSEG - 9 && s < NSEG - 6) {
      const unsigned* src = (const unsigned*)(w2 + (size_t)(w * 448 + 224 + l) * S_ + nb2 * 64);
      const unsigned sv = src[0] ^ src[64 * S_ / 2] ^ src[128 * S_ / 2] ^ (l < 32 ? src[192 * S_ / 2] : 0u);
      asm volatile("" :: "v"(sv));
    }
    wait_vm<0>();
    RAW_BARRIER();
    if (w == 0) arrive(ws, SY_CD + s * 64, SY_CD_F + s * 256, 16, l);
  }
}

DEVI void wave_sum4(float& a, float& b, float& c, float& d) {
#define STEP(F) a = F(a); b = F(b); c = F(c); d = F(d);
#define R8(v) (v + dppf<0x128>(v))
#define R4(v) (v + dppf<0x124>(v))
#define R2(v) (v + dppf<0x122>(v))
#define R1(v) (v + dppf<0x121>(v))
  STEP(R8) STEP(R4) STEP(R2) STEP(R1) STEP(xsum16) STEP(xsum32)
#undef STEP
#undef R8
#undef R4
#undef R2
#undef R1
}
DEVI void ln_worker(int j, const u16* x, unsigned char* ws, rsrc_t R) {
  const int w = __builtin_amdgcn_readfirstlane(threadIdx.x >> 6), l = threadIdx.x & 63, c = l & 15, g = l >> 4;
  const float* mod = (const float*)(ws + WS_MOD);
  const int dq = (c >> 2) * 32 + (c & 3) * 4 + (g >> 1) * 16 + (g & 1) * 2;
  float ga[8], cb[8], sb[8];
  float sbm = 0;
#pragma unroll
  for (int e = 0; e < 8; ++e) { const int d = dq + (e >> 1) * 128 + (e & 1); ga[e] = mod[1024 + d]; sb[e] = mod[1536 + d]; cb[e] = rbf(1.f + mod[2048 + d]); sbm += sb[e]; }
  sbm = wave_sum(sbm) * (1.f / 512.f);
  float sb2 = 0;
#pragma unroll
  for (int e = 0; e < 8; ++e) { sb[e] -= sbm; sb2 += sb[e] * sb[e]; }
  sb2 = wave_sum(sb2);
  u32x4 sel;
  {
    const int e = 2 * (c >> 2) + (c & 1);
#pragma unroll
    for (int i = 0; i < 4; ++i) sel[i] = ((c & 3) < 2 && (e >> 1) == i) ? ((e & 1) ? 0x3F800000u : 0x3F80u) : 0u;
  }
  st128s(R, (unsigned)WS_H2 + ((60 + (j >> 3)) * 32 + (j & 7) * 4 + w) * 1024 + l * 16, (u32x4){SENT, SENT, SENT, SENT});
  bool dead = false;
  for (int s = j >> 3; s < NSEG; s += 4) {
    const int tok = s * 32 + (j & 7) * 4 + w;
    unsigned xw[4];
#pragma unroll
    for (int q = 0; q < 4; ++q) xw[q] = *(const unsigned*)(x + (size_t)tok * 512 + dq + q * 128);
    wait_flag(R, SY_CD_F + (s > 60 ? s - 3 : s) * 256, dead, 1);
    if (s > 60) wait_flag(R, SY_HINT + (s - 60) * 256, dead, 0);
    TSTAMP(13);
    u32x4 p[4][4];
    for (int it = 0;; ++it) {
      MEMFENCE_C();
#pragma unroll
      for (int i = 0; i < 4; ++i)
#pragma unroll
        for (int q = 0; q < 4; ++q) p[i][q] = ld128s(R, (unsigned)WS_PP + (tok * 16 + 4 * i + g) * 1024 + q * 256 + c * 16);
      SCHED_FENCE();
      unsigned mx = 0;
#pragma unroll
      for (int i = 0; i < 4; ++i)
#pragma unroll
        for (int q = 0; q < 4; ++q) mx = max(mx, max4(p[i][q]));
      if (__all(mx != SENT) || dead) break;
      if (it > SPIN_CAP) dead = true;
    }
    SCHED_FENCE();
    f32x4 acc[4];
#pragma unroll
    for (int i = 0; i < 4; ++i)
#pragma unroll
      for (int q = 0; q < 4; ++q) acc[q] = mfma32(sel, p[i][q], i == 0 ? (f32x4){0, 0, 0, 0} : acc[q]);
    float v[8], sacc = 0;
#pragma unroll
    for (int q = 0; q < 4; ++q) {
      const unsigned pk = pk2(bflo(xw[q]) + ga[2 * q] * acc[q][0], bfhi(xw[q]) + ga[2 * q + 1] * acc[q][1]);
      __builtin_amdgcn_raw_buffer_store_b32(pk, R, (unsigned)WS_X1 + tok * 1024 + q * 256 + c * 16 + g * 4, 0, 16);
      v[2 * q] = bflo(pk); v[2 * q + 1] = bfhi(pk); sacc += v[2 * q] + v[2 * q + 1];
    }
    TSTAMP(14);
    const float mean = wave_sum(sacc) * (1.f / 512.f);
    float q0 = 0, q1 = 0, q2 = 0, q3 = 0;
#pragma unroll
    for (int e = 0; e < 8; ++e) { const float u = v[e] - mean; q0 += u * u; v[e] = u * cb[e]; q1 += v[e]; q2 += v[e] * v[e]; q3 += v[e] * sb[e]; }
    wave_sum4(q0, q1, q2, q3);
    if (s >= 60 && (j & 7) == 7 && w == 3) raise_hint(ws, 4 + s - 60, l);
    const float r1 = rsqrtf(q0 * (1.f / 512.f) + 1e-5f);
    const float m2 = r1 * q1 * (1.f / 512.f);
    const float var2 = (r1 * r1 * q2 + 2.f * r1 * q3 + sb2) * (1.f / 512.f) - m2 * m2;
    const float r2 = rsqrtf(fmaxf(var2, 0.f) + 1e-5f);
#pragma unroll
    for (int q = 0; q < 4; ++q)
      __builtin_amdgcn_raw_buffer_store_b32(pk2((v[2 * q] * r1 + sb[2 * q] - m2) * r2, (v[2 * q + 1] * r1 + sb[2 * q + 1] - m2) * r2), R,
                                            (unsigned)WS_H2 + tok * 1024 + q * 256 + c * 16 + g * 4, 0, 16);
    TSTAMP(15);
    wait_vm<0>();
    arrive(ws, SY_CL_C + s * 64, SY_CL + s * 256, 32, l);
  }
}

DEVI float gelu_tanh(float x) {
  const float t = fmaf(x * x, -0.10294324f, -2.3022082f);
  return x * __builtin_amdgcn_rcpf(1.f + __builtin_amdgcn_exp2f(t * x));
}
DEVI void e_worker(int mp, int nb, int mp2, const u16* w1, const u16* b1, unsigned char* ws, rsrc_t R) {
  const int w = __builtin_amdgcn_readfirstlane(threadIdx.x >> 6), l = threadIdx.x & 63, g = l >> 4, q4 = (l >> 2) & 3, p4 = l & 3, wm = w >> 1, wn = w & 1;
  constexpr unsigned LW = 0, LX = 32768;
  f32x4 acc[2][4];
#pragma unroll
  for (int m = 0; m < 2; ++m)
#pragma unroll
    for (int n = 0; n < 4; ++n) acc[m][n] = (f32x4){0, 0, 0, 0};
  const unsigned vw = (q4 * F_ + g * 32 + (p4 & 1) * 8) * 2;
  const unsigned vx = ((2 * w + (g >> 1)) * 4 + q4) * 1024 + ((g & 1) * 32 + (((p4 >> 1) ^ ((g >> 1) & 1)) * 16) + (p4 & 1) * 8) * 2;
  unsigned rw = LW + g * 1024 + q4 * 64 + p4 * 8 + wn * 512;
  const unsigned rx = LX + g * 512 + q4 * 64 + p4 * 8 + wm * 256;
  auto kstep = [&](int k) {
    u32x4 xf[2], wf[4];
#pragma unroll
    for (int m = 0; m < 2; ++m) { const unsigned a = rx + ((m ^ (g & 1)) * 32) + k * 4096; const u32x2 lo = trread(a), hi = trread(a + 2048); xf[m] = (u32x4){lo[0], lo[1], hi[0], hi[1]}; }
#pragma unroll
    for (int n = 0; n < 4; ++n) { const unsigned a = rw + (((n & 1) ^ (g & 1)) * 32) + (n >> 1) * 256 + k * 8192; const u32x2 lo = trread(a), hi = trread(a + 4096); wf[n] = (u32x4){lo[0], lo[1], hi[0], hi[1]}; }
#pragma unroll
    for (int m = 0; m < 2; ++m)
#pragma unroll
      for (int n = 0; n < 4; ++n) acc[m][n] = mfma32(wf[n], xf[m], acc[m][n]);
    MFMA_DRAIN();
  };
  u32x2 bias[2][2];
#pragma unroll
  for (int pr = 0; pr < 2; ++pr) { const int f0 = nb * 128 + wn * 64 + pr * 32 + 4 * g; bias[pr][0] = *(const u32x2*)(b1 + f0); bias[pr][1] = *(const u32x2*)(b1 + f0 + 16); }
#pragma unroll
  for (int m = 0; m < 2; ++m)
#pragma unroll
    for (int n = 0; n < 4; ++n) { const u32x2 bv = bias[n >> 1][n & 1]; acc[m][n] = (f32x4){bflo(bv[0]), bfhi(bv[0]), bflo(bv[1]), bfhi(bv[1])}; }
#pragma unroll
  for (int i = 0; i < 4; ++i) st128s(R, (unsigned)WS_UF + (((mp * 4 + wm * 2 + (i & 1)) * 56 + nb * 4 + wn * 2 + (i >> 1)) * 64 + l) * 16, (u32x4){SENT, SENT, SENT, SENT});
  wait_vm<0>();
  RAW_BARRIER();
  if (threadIdx.x == 0) __hip_atomic_fetch_add((GAS unsigned*)(ws + WS_SYNC + SY_U + mp * 64), 1u, __ATOMIC_RELAXED, __HIP_MEMORY_SCOPE_AGENT);
  bool dead = false;
  wait_word(R, (unsigned)(WS_SYNC + SY_U) + mp2 * 64, dead, 4, [](unsigned v) { return v >= 14; });
  auto issue_w = [&](int p) {
    const unsigned char* wsrc = (const unsigned char*)(w1 + (size_t)(p * 128 + w * 32) * F_ + nb * 128);
#pragma unroll
    for (int i = 0; i < 8; ++i) gldsS<false>(vw + i * 4 * F_ * 2 + (((p4 >> 1) ^ (i & 1)) * 32), wsrc, LW + (p & 1) * 65536 + (w * 8 + i) * 1024);
  };
  issue_w(0);
  for (int p = 0; p < 16; ++p, rw ^= 65536) {
    if (p < 15) issue_w(p + 1);
    const unsigned char* xsrc = ws + WS_H2 + (size_t)p * 128 * 1024 + mp * 128;
    if (p < 15) {
#pragma unroll 1
      for (int k = 0; k < 4; ++k) {
        if (w == 0) wait_flag(R, SY_CL + (p * 4 + k) * 256, dead, 4);
        RAW_BARRIER();
        gldsS<true>(vx, xsrc + k * 32768, LX + k * 4096 + w * 1024);
      }
      wait_vm<0>(); RAW_BARRIER();
#pragma unroll
      for (int k = 0; k < 4; ++k) kstep(k);
      RAW_BARRIER();
    } else {
#pragma unroll 1
      for (int k = 0; k < 4; k += 2) {
        wait_flag(R, SY_HINT + (5 + k) * 256, dead, k ? 0 : 2);
        u32x4 xa, xb;
        for (int it = 0;; ++it) {
          MEMFENCE_C();
          xa = ld128s(R, (unsigned)WS_H2 + (15 * 128 + k * 32) * 1024 + mp * 128 + vx);
          xb = ld128s(R, (unsigned)WS_H2 + (15 * 128 + k * 32 + 32) * 1024 + mp * 128 + vx);
          if (__all(max(max4(xa), max4(xb)) != SENT) || dead) break;
          if (it > SPIN_CAP) dead = true;
          __builtin_amdgcn_s_sleep(1);
        }
        TSTAMP(13);
        ldsw128(LX + k * 4096 + w * 1024 + l * 16, xa);
        ldsw128(LX + k * 4096 + 4096 + w * 1024 + l * 16, xb);
        wait_vm<0>(); RAW_BARRIER();
        kstep(k); kstep(k + 1);
        TSTAMP(14);
      }
    }
  }
#pragma unroll
  for (int pr = 0; pr < 2; ++pr)
#pragma unroll
    for (int m = 0; m < 2; ++m) {
      const f32x4 a = acc[m][2 * pr], c = acc[m][2 * pr + 1];
      if (pr == 1 && m == 1 && nb == 13 && w == 0) raise_hint(ws, 8 + mp, l);
      u32x4 o = {pk2(gelu_tanh(a[0]), gelu_tanh(a[1])), pk2(gelu_tanh(a[2]), gelu_tanh(a[3])), pk2(gelu_tanh(c[0]), gelu_tanh(c[1])), pk2(gelu_tanh(c[2]), gelu_tanh(c[3]))};
      st128s(R, (unsigned)WS_UF + (((mp * 4 + wm * 2 + m) * 56 + nb * 4 + wn * 2 + pr) * 64 + l) * 16, o);
    }
  TSTAMP(15);
  RAW_BARRIER();
}

DEVI int fresh_lane() { int l; asm volatile("v_mbcnt_lo_u32_b32 %0, -1, 0\n\tv_mbcnt_hi_u32_b32 %0, -1, %0" : "=v"(l)); return l; }
constexpr unsigned G2L = 49152;
DEVI void g2_issue(int half, int nb, int w, const u16* w2) {
  const int l = fresh_lane(), g = l >> 4, q4 = (l >> 2) & 3, p4 = l & 3;
  const unsigned vo = (((g >> 1) * 4 + q4) * S_ + (g & 1) * 32 + (((p4 >> 1) ^ (g >> 1)) * 16) + (p4 & 1) * 8) * 2;
  const unsigned char* src = (const unsigned char*)(w2 + (size_t)(w * 448 + half * 224) * S_ + nb * 64);
#pragma unroll 4
  for (int i = 0; i < 28; ++i) gldsS<false>(vo, src + (size_t)i * 8 * S_ * 2, G2L + w * 28672 + i * 1024);
}
template <int HALF> DEVI void g2_read(u32x4 (&wf)[14][4], int w) {
  const int l = fresh_lane(), g = l >> 4, q4 = (l >> 2) & 3, p4 = l & 3;
  wait_vm<0>();
#pragma unroll
  for (int n = 0; n < 4; ++n) {
    const int nt = (n + w) & 3;
    const unsigned rb = G2L + w * 28672 + g * 512 + q4 * 64 + p4 * 8 + (nt >> 1) * 256 + (((nt & 1) ^ (g & 1)) * 32);
#pragma unroll
    for (int k = 0; k < 7; ++k) {
      const u32x2 lo = trread(rb + k * 4096), hi = trread(rb + k * 4096 + 2048);
      wf[HALF * 7 + k][n] = (u32x4){lo[0], lo[1], hi[0], hi[1]};
    }
  }
  asm volatile("s_waitcnt lgkmcnt(0)" ::: "memory");
}
DEVI void g2_load(u32x4 (&wf)[14][4], int nb, int w, const u16* w2, bool first_issued) {
  if (!first_issued) { RAW_BARRIER(); g2_issue(0, nb, w, w2); }
  g2_read<0>(wf, w);
  g2_issue(1, nb, w, w2);
  g2_read<1>(wf, w);
}
DEVI void g2_run(const u32x4 (&wf)[14][4], int mp, int nb, int w, const u16* b2, unsigned char* ws, u16* out, bool uknown) {
  const rsrc_t R = __builtin_amdgcn_make_buffer_rsrc(ws, 0, (unsigned)WS_END, 0x00020000);
  const int l = fresh_lane(), c = l & 15, g = l >> 4;
  const int s = nb * 64 + w * 16 + c;
  bool dead = false;
  if (!uknown) wait_word(R, (unsigned)(WS_SYNC + SY_U) + mp * 64, dead, 4, [](unsigned v) { return v >= 14; });
  const float bias = bf2f(b2[s]);
  f32x4 gb[4];
#pragma unroll
  for (int m = 0; m < 4; ++m) gb[m] = *(const f32x4*)((const float*)(ws + WS_MOD) + 2560 + mp * 64 + 32 * (m >> 1) + (2 * (m & 1) + (g >> 1)) * 4 + (g & 1) * 16);
  const unsigned ub = (unsigned)WS_UF + ((mp * 4 * 56 + w * 14) * 64 + l) * 16;
  TSTAMP2(8);
  if (!uknown) {
    if (w == 0) wait_flag(R, SY_HINT + (8 + mp) * 256, dead, 1);
    RAW_BARRIER();
  }
  {
    const unsigned pa = l < 56 ? (unsigned)WS_UF + (((mp * 4 + l / 14) * 56 + w * 14 + l % 14) * 64 + 63) * 16
                               : (unsigned)(WS_SYNC + SY_CL) + (nb * 2 + (w >> 1)) * 256 + (mp & 3) * 64;
    for (int it = 0; !dead; ++it) {
      MEMFENCE_C();
      const unsigned v = ld32s(R, pa);
      if (__all(l < 56 ? v != SENT : v != 0)) break;
      if (it > SPIN_CAP) dead = true;
      __builtin_amdgcn_s_sleep(2);
    }
  }
  MEMFENCE_C();
  TSTAMP2(9);
  u32x2 xv[4];
#pragma unroll
  for (int m = 0; m < 4; ++m) xv[m] = ld64s(R, (unsigned)WS_X1 + s * 1024 + (mp * 64 + m * 16 + 4 * g) * 2);
  constexpr int RD = 6;
  f32x4 acc[4][4];
  for (int it = 0;; ++it) {
    MEMFENCE_C();
    u32x4 xf[RD][4];
#pragma unroll
    for (int k = 0; k < RD; ++k)
#pragma unroll
      for (int m = 0; m < 4; ++m) xf[k][m] = ld128s(R, ub + (m * 56 + k) * 1024);
    SCHED_FENCE();
    unsigned mx = 0;
#pragma unroll
    for (int k = 0; k < 14; ++k) {
#pragma unroll
      for (int m = 0; m < 4; ++m) {
#pragma unroll
        for (int n = 0; n < 4; ++n) acc[m][n] = mfma32(xf[k % RD][m], wf[k][n], k == 0 ? (f32x4){0, 0, 0, 0} : acc[m][n]);
        mx = max(mx, max4(xf[k % RD][m]));
        if (k + RD < 14) xf[k % RD][m] = ld128s(R, ub + (m * 56 + k + RD) * 1024);
        SCHED_FENCE();
      }
    }
    MFMA_DRAIN();
    if (__all(mx != SENT) || dead) break;
    if (it > SPIN_CAP) dead = true;
  }
  MFMA_DRAIN();
  TSTAMP2(10);
#pragma unroll
  for (int n = 1; n < 4; ++n)
#pragma unroll
    for (int m = 0; m < 4; ++m) ldsw128(((((n + w) & 3) * 3 + (n - 1)) * 4 + m) * 1024 + l * 16, __builtin_bit_cast(u32x4, acc[m][n]));
  RAW_BARRIER();
#pragma unroll
  for (int m = 0; m < 4; ++m) {
    f32x4 a = acc[m][0];
#pragma unroll
    for (int i = 0; i < 3; ++i) a += __builtin_bit_cast(f32x4, ldsr128(((w * 3 + i) * 4 + m) * 1024 + l * 16));
    const int d0 = mp * 64 + 32 * (m >> 1) + (2 * (m & 1) + (g >> 1)) * 4 + (g & 1) * 16;
    *(u32x2*)(out + (size_t)s * 512 + d0) = (u32x2){pk2(bflo(xv[m][0]) + gb[m][0] * (a[0] + bias), bfhi(xv[m][0]) + gb[m][1] * (a[1] + bias)),
                                                    pk2(bflo(xv[m][1]) + gb[m][2] * (a[2] + bias), bfhi(xv[m][1]) + gb[m][3] * (a[3] + bias))};
  }
  TSTAMP2(11);
}

DEVI u16* karg(int i) {
  const __attribute__((address_space(4))) char* p = (const __attribute__((address_space(4))) char*)__builtin_amdgcn_kernarg_segment_ptr();
  asm volatile("" : "+s"(p));
  return (u16*)(GAS u16*)(*(const __attribute__((address_space(4))) unsigned long long*)(p + 8 * i));
}
#define XIN karg(0)
#define Wq karg(1)
#define Wk karg(2)
#define Wv karg(3)
#define Wo karg(4)
#define w1 karg(5)
#define b1 karg(6)
#define w2 karg(7)
#define b2 karg(8)
#define out karg(9)
__global__ void __launch_bounds__(256, 1) k_mega(const u16* a0, const u16* a1, const u16* a2, const u16* a3, const u16* a4, const u16* a5, const u16* a6, const u16* a7,
                                                 const u16* a8, u16* a9, unsigned char* __restrict__ ws, int pd1, int pd2, int t3, int t4) {
  extern __shared__ __attribute__((aligned(16))) unsigned char smem[];
  const int b = blockIdx.x;
  const unsigned* cfg = (const unsigned*)(ws + WS_CFG);
  const int off = cfg[0] & 7;
  const int px = (b + off - (off & 4)) & 7;
  const int j = b >> 3;
  TSTAMP(0);
  const int wv = __builtin_amdgcn_readfirstlane(threadIdx.x >> 6);
  if (px < 2 && j < 16) {
    rec_body(Wq, Wk, Wv, Wo, ws, j * 2 + px, pd1, pd2);
    u32x4 wf[14][4];
    g2_load(wf, j, wv, w2, false);
    g2_run(wf, px, j, wv, b2, ws, out, false);
    return;
  }
  const rsrc_t R = __builtin_amdgcn_make_buffer_rsrc(ws, 0, (unsigned)WS_END, 0x00020000);
  if (px < 2) {
    if (px == 0 && j == 16) {
      const int w = threadIdx.x >> 6;
      unsigned best[2] = {~0u, ~0u};
      unsigned* p = (unsigned*)(ws + WS_EXCH + 524288 + w * 2048 + (threadIdx.x & 15) * 4);
#pragma unroll 1
      for (int it = 0; it < 16; ++it) {
        unsigned* q = p + (it & 1) * 2048 + (it >> 1) * 64;
        unsigned long long t0, t1;
        asm volatile("s_memtime %0\n\ts_waitcnt lgkmcnt(0)\n\tglobal_store_dword %2, %3, off sc1\n\ts_waitcnt vmcnt(0)\n\ts_memtime %1\n\ts_waitcnt lgkmcnt(0)"
                     : "=&s"(t0), "=&s"(t1) : "v"(q), "v"(SENT) : "memory");
        const unsigned d = (unsigned)(t1 - t0);
        if (it & 1) best[1] = min(best[1], d); else best[0] = min(best[0], d);
      }
      LAS unsigned* lat = (LAS unsigned*)0;
      if ((threadIdx.x & 63) == 0) { lat[w * 2] = best[0]; lat[w * 2 + 1] = best[1]; }
      RAW_BARRIER();
      if (threadIdx.x == 0) {
        const unsigned l0 = min(min(lat[0], lat[2]), min(lat[4], lat[6])), l1 = min(min(lat[1], lat[3]), min(lat[5], lat[7]));
        __hip_atomic_store((GAS unsigned*)(ws + WS_SYNC + SY_POL), l1 < l0 ? 2u : 1u, __ATOMIC_RELAXED, __HIP_MEMORY_SCOPE_AGENT);
      }
    }
    const int h = (j - 16) * 2 + px, l = threadIdx.x & 63;
    bool dead = false;
    unsigned sink = 0;
    const unsigned flbase = get_flbase(R, dead);
    for (int s = 1; s < NSEG && wv == 0; ++s) {
      if ((s & 1) == 0 || s == 1) {
#pragma unroll
        for (int m = 0; m < 2; ++m) wait_word(R, (unsigned)WS_SYNC + (m * 32 + h) * 256 + 64, dead, 2, [&](unsigned v) { return (v >> (s >> 1)) & 1; });
      }
      if (s >= 10) wait_step_hint(R, flbase, h, s - 6, dead, 4);
      const unsigned rec = (unsigned)WS_KV + (h * 64 + s) * 16384 + l * 128;
      sink ^= ld32s(R, rec) ^ ld32s(R, rec + 8192);
    }
    asm volatile("" :: "v"(sink));
    if (wv == 0) { wait_step_hint(R, flbase, h, NSEG - 1, dead, 4); __builtin_amdgcn_s_sleep(32); }
    u32x4 wf[14][4];
    g2_load(wf, j, wv, w2, false);
    g2_run(wf, px, j, wv, b2, ws, out, false);
    return;
  }
  const int xw = px - 2;
  {
    const int wi = j * 6 + xw;
    unsigned sv = 0;
    if (wi < 64) {
#pragma unroll
      for (int m = 0; m < 4; ++m) if ((t3 >> m) & 1) {
        const unsigned* src = (const unsigned*)(m == 0 ? Wq : m == 1 ? Wk : m == 2 ? Wv : Wo) + wi * 16384 + threadIdx.x * 32;
        sv ^= src[0] ^ src[8192];
      }
    }
    if (wi >= 64) ln1_tile(191 - wi, XIN, ws, R);
    TSTAMP(6);
    asm volatile("" :: "v"(sv));
  }
  {
    const int nkv = xw < 2 ? 6 : 5, kv0 = xw < 2 ? xw * 6 : 12 + (xw - 2) * 5, q0 = xw < 2 ? xw * 2 : 4 + (xw - 2) * 3;
#pragma unroll 1
    for (int u = j; u < nkv * 8; u += 32) {
      const int c2 = u % (nkv * 2), cb = kv0 + (c2 >> 1);
      kv_fast(cb & 1, (cb >> 1) * 2 + (c2 & 1), u / (nkv * 2), Wk, Wv, ws, R);
      if (u < 32) TSTAMP(7);
    }
    TSTAMP(1);
#pragma unroll 1
    for (int u = 31 - j; u < 128 && j >= 8; u += 24) {
      const int p = u >> 3, i = u & 7;
      if (i < nkv && p >= 14) continue;
      int mat, nb, mp;
      if (i < nkv) { const int cb = kv0 + i; mp = p + 2; mat = cb & 1; nb = cb >> 1; }
      else { mp = p; mat = 2; nb = q0 + i - nkv; }
      qkv_unit(mat, nb, mp, Wq, Wk, Wv, ws, R);
      if (u < 48) TSTAMP(2 + (u >= 24));
    }
    TSTAMP(4);
  }
  const int grp = xw * 3 + (j >> 3) - 1;
  u32x4 wf[14][4];
  if (j < 8) cd_worker((xw * 8 + j) & 15, (xw * 8 + j) >> 4, Wo, w2, j, ws, R);
  TSTAMP(5);
  g2_load(wf, j, wv, w2, j < 8);
  if (j >= 8) {
    if (grp < 14) e_worker(j & 7, grp, px, w1, b1, ws, R);
    else ln_worker((grp - 14) * 8 + (j & 7), XIN, ws, R);
    TSTAMP(5);
  }
  g2_run(wf, px, j, wv, b2, ws, out, j >= 8 && grp < 14);
  TSTAMP(12);
}

#undef XIN
#undef Wq
#undef Wk
#undef Wv
#undef Wo
#undef w1
#undef b1
#undef w2
#undef b2
#undef out

#define POL 0
#define PFB 192
#define PD1 2460
#define PD2 1080

extern "C" void kernel_launch(void* const* d_in, const int* in_sizes, int n_in, void* d_out, int out_size, void* d_ws, size_t ws_size, hipStream_t stream) {
  const u16* x = (const u16*)d_in[0];
  const u16* mem0 = (const u16*)d_in[1];
  const u16* Wq = (const u16*)d_in[2];
  const u16* Wk = (const u16*)d_in[3];
  const u16* Wv = (const u16*)d_in[4];
  const u16* Wo = (const u16*)d_in[5];
  const u16* w1 = (const u16*)d_in[8];
  const u16* b1 = (const u16*)d_in[9];
  const u16* w2 = (const u16*)d_in[10];
  const u16* b2 = (const u16*)d_in[11];
  const u16* c = (const u16*)d_in[12];
  const u16* wmod = (const u16*)d_in[13];
  unsigned char* ws = (unsigned char*)(((size_t)d_ws + 2 * MB - 1) & ~(size_t)(2 * MB - 1));
  (void)hipFuncSetAttribute((const void*)k_mega, hipFuncAttributeMaxDynamicSharedMemorySize, RL_END);
  k_mod<<<160 + PFB, 256, 0, stream>>>(c, wmod, mem0, ws, x, Wq, Wk, Wv, Wo);
  k_mega<<<256, 256, RL_END, stream>>>(x, Wq, Wk, Wv, Wo, w1, b1, w2, b2, (u16*)d_out, ws, PD1, PD2, 8, 0);
}
```
